```python
import torch

def _softmax(x, axis=-1):
    return torch.softmax(x, dim=axis)
def _gelu(x):
    return torch.nn.functional.gelu(x, approximate="tanh")
def _silu(x):
    return torch.nn.functional.silu(x)
def _sigmoid(x):
    return torch.sigmoid(x)
def _layernorm(x, axis=-1, eps=1e-5):
    m = x.mean(dim=axis, keepdim=True)
    v = x.var(dim=axis, keepdim=True, unbiased=False)
    return (x - m) / torch.sqrt(v + eps)
def _scatter_add(tgt, idx, src, axis=0):
    return tgt.clone().index_add_(axis, idx, src)
def _segment_sum(src, seg, n):
    out = torch.zeros((n,) + src.shape[1:], dtype=src.dtype)
    return out.index_add_(0, seg, src)
def _pad(x, pw):
    flat = tuple(v for pair in reversed(pw) for v in pair)
    return torch.nn.functional.pad(x, flat)

kernel_name = 'standalone__spectral_fnet__prompt'

def setup_inputs():

    _w = (lambda a: (lambda t: t.to(torch.bfloat16) if t.is_floating_point() else t)(torch.as_tensor(a)))
    B, S, D = 1, 2048, 576
    ns = torch.arange(S)[:, None] * torch.arange(S)[None, :]
    nd = torch.arange(D)[:, None] * torch.arange(D)[None, :]
    Ws_re = torch.cos(-2.0 * torch.pi * ns / S).to(float)
    Ws_im = torch.sin(-2.0 * torch.pi * ns / S).to(float)
    Wd_re = torch.cos(-2.0 * torch.pi * nd / D).to(float)
    Wd_im = torch.sin(-2.0 * torch.pi * nd / D).to(float)
    return {
        "x": _w((lambda *s: torch.randn(*s, dtype=torch.bfloat16) * 0.05)(B, S, D)),
        "dft_seq_re": _w(Ws_re),
        "dft_seq_im": _w(Ws_im),
        "dft_hid_re": _w(Wd_re),
        "dft_hid_im": _w(Wd_im),
    }

def reference(x, dft_seq_re, dft_seq_im, dft_hid_re, dft_hid_im):
    a = torch.einsum("bsd,ed->bse", x, dft_hid_re)
    b = torch.einsum("bsd,ed->bse", x, dft_hid_im)
    y = torch.einsum("ts,bse->bte", dft_seq_re, a) - torch.einsum("ts,bse->bte", dft_seq_im, b)
    return y
```

```cpp
#include <hip/hip_runtime.h>
#include <hip/hip_bf16.h>

typedef __attribute__((ext_vector_type(8))) short bf16x8;
typedef __attribute__((ext_vector_type(4))) float f32x4;
typedef unsigned short u16;

#define S_ 2048
#define D_ 576

__device__ __forceinline__ u16 f2bf(float f) {
    __hip_bfloat16 h = __float2bfloat16(f);
    return *reinterpret_cast<u16*>(&h);
}

__global__ __launch_bounds__(256) void k1(const u16* __restrict__ x, const u16* __restrict__ wre, const u16* __restrict__ wim,
                                          u16* __restrict__ abT) {
    int wid = threadIdx.x >> 6, lane = threadIdx.x & 63;
    int r = lane & 15, g = lane >> 4;
    int e0 = blockIdx.x * 64 + (wid >> 1) * 32;
    int s0 = blockIdx.y * 64 + (wid & 1) * 32;
    bool im = e0 >= D_;
    const u16* W = im ? (wim + (size_t)(e0 - D_) * D_) : (wre + (size_t)e0 * D_);
    f32x4 acc[2][2] = {};
    for (int k = 0; k < D_; k += 32) {
        bf16x8 a[2], b[2];
        for (int i = 0; i < 2; ++i) a[i] = *(const bf16x8*)(x + (size_t)(s0 + i * 16 + r) * D_ + k + g * 8);
        for (int j = 0; j < 2; ++j) b[j] = *(const bf16x8*)(W + (size_t)(j * 16 + r) * D_ + k + g * 8);
        for (int i = 0; i < 2; ++i)
            for (int j = 0; j < 2; ++j)
                acc[i][j] = __builtin_amdgcn_mfma_f32_16x16x32_bf16(a[i], b[j], acc[i][j], 0, 0, 0);
    }
    float sg = im ? -1.f : 1.f;
    for (int i = 0; i < 2; ++i)
        for (int j = 0; j < 2; ++j) {
            ushort4 v;
            v.x = f2bf(sg * acc[i][j][0]); v.y = f2bf(sg * acc[i][j][1]); v.z = f2bf(sg * acc[i][j][2]); v.w = f2bf(sg * acc[i][j][3]);
            *(ushort4*)(abT + (size_t)(e0 + j * 16 + r) * S_ + s0 + i * 16 + g * 4) = v;
        }
}

__global__ __launch_bounds__(256) void k2(const u16* __restrict__ sre, const u16* __restrict__ sim, const u16* __restrict__ abT,
                                          u16* __restrict__ y) {
    int wid = threadIdx.x >> 6, lane = threadIdx.x & 63;
    int r = lane & 15, g = lane >> 4;
    int t0 = blockIdx.y * 64 + (wid >> 1) * 32;
    int e0 = blockIdx.x * 64 + (wid & 1) * 32;
    f32x4 acc[2][2] = {};
    for (int h = 0; h < 2; ++h) {
        const u16* A = h ? sim : sre;
        const u16* B = abT + (size_t)h * D_ * S_;
        for (int k = 0; k < S_; k += 32) {
            bf16x8 a[2], b[2];
            for (int i = 0; i < 2; ++i) a[i] = *(const bf16x8*)(B + (size_t)(e0 + i * 16 + r) * S_ + k + g * 8);
            for (int j = 0; j < 2; ++j) b[j] = *(const bf16x8*)(A + (size_t)(t0 + j * 16 + r) * S_ + k + g * 8);
            for (int i = 0; i < 2; ++i)
                for (int j = 0; j < 2; ++j)
                    acc[i][j] = __builtin_amdgcn_mfma_f32_16x16x32_bf16(a[i], b[j], acc[i][j], 0, 0, 0);
        }
    }
    for (int i = 0; i < 2; ++i)
        for (int j = 0; j < 2; ++j) {
            ushort4 v;
            v.x = f2bf(acc[i][j][0]); v.y = f2bf(acc[i][j][1]); v.z = f2bf(acc[i][j][2]); v.w = f2bf(acc[i][j][3]);
            *(ushort4*)(y + (size_t)(t0 + j * 16 + r) * D_ + e0 + i * 16 + g * 4) = v;
        }
}

extern "C" void kernel_launch(void* const* d_in, const int* in_sizes, int n_in,
                              void* d_out, int out_size, void* d_ws, size_t ws_size,
                              hipStream_t stream) {
    const u16* x = (const u16*)d_in[0];
    const u16* sre = (const u16*)d_in[1];
    const u16* sim = (const u16*)d_in[2];
    const u16* wre = (const u16*)d_in[3];
    const u16* wim = (const u16*)d_in[4];
    u16* abT = (u16*)d_ws;
    k1<<<dim3(2 * D_ / 64, S_ / 64), 256, 0, stream>>>(x, wre, wim, abT);
    k2<<<dim3(D_ / 64, S_ / 64), 256, 0, stream>>>(sre, sim, abT, (u16*)d_out);
}
```

```cpp
#include <hip/hip_runtime.h>
#include <hip/hip_bf16.h>

typedef __attribute__((ext_vector_type(8))) short bf16x8;
typedef __attribute__((ext_vector_type(4))) float f32x4;
typedef unsigned short u16;

#define S_ 2048
#define D_ 576

__device__ __forceinline__ u16 f2bf(float f) {
    __hip_bfloat16 h = __float2bfloat16(f);
    return *reinterpret_cast<u16*>(&h);
}

__global__ __launch_bounds__(256) void k1(const u16* __restrict__ x, const u16* __restrict__ wre, const u16* __restrict__ wim,
                                          u16* __restrict__ abT) {
    int wid = threadIdx.x >> 6, lane = threadIdx.x & 63;
    int r = lane & 15, g = lane >> 4;
    int e0 = blockIdx.x * 64 + (wid >> 1) * 32;
    int s0 = blockIdx.y * 64 + (wid & 1) * 32;
    bool im = e0 >= D_;
    const u16* W = im ? (wim + (size_t)(e0 - D_) * D_) : (wre + (size_t)e0 * D_);
    f32x4 acc[2][2] = {};
    for (int k = 0; k < D_; k += 32) {
        bf16x8 a[2], b[2];
        for (int i = 0; i < 2; ++i) a[i] = *(const bf16x8*)(x + (size_t)(s0 + i * 16 + r) * D_ + k + g * 8);
        for (int j = 0; j < 2; ++j) b[j] = *(const bf16x8*)(W + (size_t)(j * 16 + r) * D_ + k + g * 8);
        for (int i = 0; i < 2; ++i)
            for (int j = 0; j < 2; ++j)
                acc[i][j] = __builtin_amdgcn_mfma_f32_16x16x32_bf16(a[i], b[j], acc[i][j], 0, 0, 0);
    }
    float sg = im ? -1.f : 1.f;
    for (int i = 0; i < 2; ++i)
        for (int j = 0; j < 2; ++j) {
            ushort4 v;
            v.x = f2bf(sg * acc[i][j][0]); v.y = f2bf(sg * acc[i][j][1]); v.z = f2bf(sg * acc[i][j][2]); v.w = f2bf(sg * acc[i][j][3]);
            *(ushort4*)(abT + (size_t)(e0 + j * 16 + r) * S_ + s0 + i * 16 + g * 4) = v;
        }
}

__global__ __launch_bounds__(256) void k2(const u16* __restrict__ sre, const u16* __restrict__ sim, const u16* __restrict__ abT,
                                          u16* __restrict__ y) {
    int wid = threadIdx.x >> 6, lane = threadIdx.x & 63;
    int r = lane & 15, g = lane >> 4;
    int t0 = blockIdx.y * 64 + (wid >> 1) * 32;
    int e0 = blockIdx.x * 64 + (wid & 1) * 32;
    f32x4 acc[2][2] = {};
    for (int h = 0; h < 2; ++h) {
        const u16* A = h ? sim : sre;
        const u16* B = abT + (size_t)h * D_ * S_;
        for (int k = 0; k < S_; k += 32) {
            bf16x8 a[2], b[2];
            for (int i = 0; i < 2; ++i) a[i] = *(const bf16x8*)(B + (size_t)(e0 + i * 16 + r) * S_ + k + g * 8);
            for (int j = 0; j < 2; ++j) b[j] = *(const bf16x8*)(A + (size_t)(t0 + j * 16 + r) * S_ + k + g * 8);
            for (int i = 0; i < 2; ++i)
                for (int j = 0; j < 2; ++j)
                    acc[i][j] = __builtin_amdgcn_mfma_f32_16x16x32_bf16(a[i], b[j], acc[i][j], 0, 0, 0);
        }
    }
    for (int i = 0; i < 2; ++i)
        for (int j = 0; j < 2; ++j) {
            ushort4 v;
            v.x = f2bf(acc[i][j][0]); v.y = f2bf(acc[i][j][1]); v.z = f2bf(acc[i][j][2]); v.w = f2bf(acc[i][j][3]);
            *(ushort4*)(y + (size_t)(t0 + j * 16 + r) * D_ + e0 + i * 16 + g * 4) = v;
        }
}


typedef __attribute__((ext_vector_type(4))) unsigned u32x4;
__global__ __launch_bounds__(512) void probe(const char* p0, const char* p1, const char* p2, const char* p3, const char* p4, int mode) {
    if (mode == 0) return;
    unsigned t = threadIdx.x * 16;
    const char* a = p1 + (size_t)blockIdx.x * 32768;
    const char* b = p2 + (size_t)blockIdx.x * 32768;
    const char* c = p0 + (size_t)blockIdx.x * 9216;
    u32x4 v;
    for (int i = 0; i < 4; ++i) {
        asm volatile("global_load_dwordx4 %0, %1, %2" : "=v"(v) : "v"(t + i * 8192), "s"(a) : "memory");
        asm volatile("global_load_dwordx4 %0, %1, %2" : "=v"(v) : "v"(t + i * 8192), "s"(b) : "memory");
    }
    asm volatile("global_load_dwordx4 %0, %1, %2" : "=v"(v) : "v"(t), "s"(c) : "memory");
    if (t < 1024) asm volatile("global_load_dwordx4 %0, %1, %2" : "=v"(v) : "v"(t + 8192), "s"(c) : "memory");
    if (t < 2592) {
        asm volatile("global_load_dwordx4 %0, %1, %2" : "=v"(v) : "v"(t), "s"(p3 + (size_t)blockIdx.x * 2592) : "memory");
        asm volatile("global_load_dwordx4 %0, %1, %2" : "=v"(v) : "v"(t), "s"(p4 + (size_t)blockIdx.x * 2592) : "memory");
    }
    asm volatile("s_waitcnt vmcnt(0)" ::: "memory");
}

extern "C" void kernel_launch(void* const* d_in, const int* in_sizes, int n_in,
                              void* d_out, int out_size, void* d_ws, size_t ws_size,
                              hipStream_t stream) {
    const u16* x = (const u16*)d_in[0];
    const u16* sre = (const u16*)d_in[1];
    const u16* sim = (const u16*)d_in[2];
    const u16* wre = (const u16*)d_in[3];
    const u16* wim = (const u16*)d_in[4];
    u16* abT = (u16*)d_ws;
    k1<<<dim3(2 * D_ / 64, S_ / 64), 256, 0, stream>>>(x, wre, wim, abT);
    k2<<<dim3(D_ / 64, S_ / 64), 256, 0, stream>>>(sre, sim, abT, (u16*)d_out);
}
```

```cpp
#include <hip/hip_runtime.h>
#include <hip/hip_bf16.h>

__global__ __launch_bounds__(256, 1) __attribute__((aligned(4096))) void fnet_fused(const void* x, const void* sre, const void* sim, const void* wre, const void* wim,
                                                     void* out, void* ws) {
    asm volatile(
        "Lstart:\n"
        "s_load_dwordx8 s[64:71], %0, 0x0\n"
        "s_load_dwordx4 s[72:75], %0, 0x20\n"
        "s_load_dwordx2 s[76:77], %0, 0x30\n"
        "s_mov_b32 s42, %1\n"
        "v_lshrrev_b32 v2, 6, v0\n"
        "s_getpc_b64 s[54:55]\n"
        "Lpc:\n"
        "s_lshr_b32 s51, s42, 3\n"
        "s_lshl_b32 s51, s51, 10\n"
        "v_and_b32 v54, 1, v0\n"
        "v_lshl_or_b32 v54, v2, 1, v54\n"
        "v_lshl_add_u32 v54, v54, 7, s51\n"
        "v_min_u32 v54, (Lend - Lpc - 4), v54\n"
        "s_mov_b64 exec, 3\n"
        "global_load_dword v249, v54, s[54:55]\n"
        "s_mov_b64 exec, -1\n"
        "v_and_b32 v1, 63, v0\n"
        "v_readfirstlane_b32 s43, v2\n"
        "s_bfe_u32 s44, s42, 0x10002\n"
        "s_bfe_u32 s45, s42, 0x20003\n"
        "s_and_b32 s21, s42, 3\n"
        "s_lshr_b32 s50, s42, 5\n"
        "s_lshl3_add_u32 s46, s21, s50\n"
        "s_lshl2_add_u32 s47, s44, s45\n"
        "s_movk_i32 s56, 0x80\n"
        "v_lshrrev_b32 v2, 3, v1\n"
        "v_and_b32 v3, 7, v1\n"
        "v_xor_b32 v53, v2, v3\n"
        "v_lshlrev_b32 v53, 4, v53\n"
        "s_lshl2_add_u32 s61, s42, s43\n"
        "s_mul_i32 s60, s43, 14336\n"
        "s_add_u32 s60, s60, 104448\n"
        "s_lshr_b32 s20, s50, 1\n"
        "s_and_b32 s49, s50, 1\n"
        "s_lshl1_add_u32 s49, s49, s45\n"
        "s_cmp_lt_u32 s45, 2\n"
        "s_cbranch_scc1 Lwork\n"
        "s_and_b32 s52, s45, 1\n"
        "s_lshl1_add_u32 s52, s50, s52\n"
        "s_lshl2_add_u32 s52, s52, s43\n"
        "s_mul_i32 s52, s52, 17\n"
        "v_add_u32 v54, s52, v1\n"
        "s_waitcnt lgkmcnt(0)\n"
        "s_cmp_eq_u32 s44, 0\n"
        "s_cselect_b64 s[26:27], s[70:71], s[72:73]\n"
        "s_mul_i32 s52, s21, 589824\n"
        "s_add_u32 s24, s64, s52\n"
        "s_addc_u32 s25, s65, 0\n"
        "s_sub_u32 s26, s26, 589824\n"
        "s_subb_u32 s27, s27, 0\n"
        "v_cmp_gt_u32 vcc, 0x200, v54\n"
        "v_mov_b32 v244, s26\n"
        "v_mov_b32 v245, s27\n"
        "v_mov_b32 v246, s24\n"
        "v_mov_b32 v247, s25\n"
        "v_cndmask_b32 v244, v244, v246, vcc\n"
        "v_cndmask_b32 v245, v245, v247, vcc\n"
        "v_mul_u32_u24 v54, 0x480, v54\n"
        "v_add_co_u32 v244, vcc, v244, v54\n"
        "s_nop 1\n"
        "v_addc_co_u32 v245, vcc, 0, v245, vcc\n"
        "s_mov_b64 exec, 0x1ffff\n"
        "global_load_dword v248, v[244:245], off offset:128\n"
        "global_load_dword v248, v[244:245], off offset:256\n"
        "global_load_dword v248, v[244:245], off offset:384\n"
        "global_load_dword v248, v[244:245], off offset:512\n"
        "global_load_dword v248, v[244:245], off offset:640\n"
        "global_load_dword v248, v[244:245], off offset:768\n"
        "global_load_dword v248, v[244:245], off offset:896\n"
        "global_load_dword v248, v[244:245], off offset:1024\n"
        "s_mov_b64 exec, -1\n"
        "s_branch Lpref\n"
        "Lwork:\n"
        "s_mov_b32 s63, 0\n"
        "v_bfe_u32 v54, v1, 4, 1\n"
        "v_xor_b32 v54, v54, v3\n"
        "s_mul_i32 s51, s43, 36864\n"
        "v_mul_u32_u24 v37, 0x480, v2\n"
        "v_add_u32 v8, s51, v37\n"
        "v_xor_b32 v55, 0, v54\n"
        "v_lshl_add_u32 v4, v55, 4, v8\n"
        "v_xor_b32 v55, 2, v54\n"
        "v_lshl_add_u32 v5, v55, 4, v8\n"
        "v_add_u32 v5, 8192, v5\n"
        "v_xor_b32 v55, 4, v54\n"
        "v_lshl_add_u32 v6, v55, 4, v8\n"
        "v_add_u32 v6, 16384, v6\n"
        "v_xor_b32 v55, 6, v54\n"
        "v_lshl_add_u32 v7, v55, 4, v8\n"
        "v_add_u32 v7, 24576, v7\n"
        "v_add_u32 v8, v8, v53\n"
        "v_add_u32 v9, 8192, v8\n"
        "v_add_u32 v10, 16384, v8\n"
        "v_add_u32 v11, 24576, v8\n"
        "s_mul_i32 s51, s43, 4608\n"
        "s_add_u32 s51, s51, 147456\n"
        "s_and_b32 s52, s43, 1\n"
        "s_lshl_b32 s52, s52, 6\n"
        "v_xor_b32 v55, s52, v53\n"
        "v_add3_u32 v37, v37, v55, s51\n"
        "s_lshl_b32 s58, s43, 12\n"
        "s_lshl_b32 s59, s43, 9\n"
        "s_lshl_b32 s18, s47, 5\n"
        "s_add_u32 s18, s18, s46\n"
        "s_lshr_b32 s51, s18, 3\n"
        "s_lshl_b32 s51, s51, 7\n"
        "s_lshl2_add_u32 s51, s50, s51\n"
        "s_add_u32 s52, s51, 0x7b0000\n"
        "v_mov_b32 v35, s52\n"
        "s_lshl_b32 s19, s18, 10\n"
        "s_add_u32 s19, s19, 0x7c0000\n"
        "s_add_u32 s62, s19, 512\n"
        "v_mov_b32 v30, s62\n"
        "s_add_u32 s62, s19, 768\n"
        "v_writelane_b32 v30, s62, 14\n"
        "s_lshl2_add_u32 s51, s44, s49\n"
        "s_lshl2_add_u32 s51, s51, s43\n"
        "s_lshl_b32 s51, s51, 13\n"
        "s_lshl2_add_u32 s62, s21, s20\n"
        "s_lshl2_add_u32 s51, s62, s51\n"
        "s_add_u32 s51, s51, 0x7bf080\n"
        "v_lshl_add_u32 v54, v1, 10, s51\n"
        "v_cmp_gt_u32 vcc, 12, v1\n"
        "s_nop 1\n"
        "v_cndmask_b32 v30, v30, v54, vcc\n"
        "s_and_b32 s51, s45, 1\n"
        "s_lshl2_add_u32 s51, s44, s51\n"
        "s_lshl2_add_u32 s51, s51, s43\n"
        "s_lshr_b32 s62, s45, 1\n"
        "s_lshl3_add_u32 s51, s51, s62\n"
        "s_lshl_b32 s51, s51, 10\n"
        "s_lshl2_add_u32 s51, s46, s51\n"
        "s_add_u32 s51, s51, 0x7c0000\n"
        "v_lshl_add_u32 v54, v1, 11, s51\n"
        "v_cmp_gt_u32 vcc, 4, v1\n"
        "s_nop 1\n"
        "v_cndmask_b32 v30, v30, v54, vcc\n"
        "v_mov_b32 v36, v30\n"
        "v_writelane_b32 v36, s52, 17\n"
        "s_lshl2_add_u32 s51, s21, s20\n"
        "s_mul_i32 s51, s51, 147456\n"
        "s_lshl_b32 s62, s63, 7\n"
        "s_add_u32 s51, s51, s62\n"
        "s_mul_i32 s52, s49, 165888\n"
        "s_add_u32 s52, s52, s62\n"
        "s_waitcnt lgkmcnt(0)\n"
        "s_mov_b64 s[30:31], s[76:77]\n"
        "global_load_dword v31, v36, s[30:31] sc1\n"
        "s_add_u32 s24, s64, s51\n"
        "s_addc_u32 s25, s65, 0\n"
        "s_cmp_eq_u32 s44, 0\n"
        "s_cselect_b64 s[26:27], s[70:71], s[72:73]\n"
        "s_add_u32 s26, s26, s52\n"
        "s_addc_u32 s27, s27, 0\n"
        "s_add_u32 m0, s59, 32768\n"
        "s_mov_b32 exec_hi, 0\n"
        "global_load_lds_dwordx4 v37, s[26:27]\n"
        "s_mov_b32 exec_hi, -1\n"
        "s_add_u32 m0, s58, 0\n"
        "s_nop 0\n"
        "global_load_lds_dwordx4 v4, s[24:25]\n"
        "global_load_lds_dwordx4 v5, s[24:25] offset:1024\n"
        "global_load_lds_dwordx4 v6, s[24:25] offset:2048\n"
        "global_load_lds_dwordx4 v7, s[24:25] offset:3072\n"
        "s_add_u32 m0, s58, 16384\n"
        "s_nop 0\n"
        "global_load_lds_dwordx4 v8, s[26:27]\n"
        "global_load_lds_dwordx4 v9, s[26:27] offset:1024\n"
        "global_load_lds_dwordx4 v10, s[26:27] offset:2048\n"
        "global_load_lds_dwordx4 v11, s[26:27] offset:3072\n"
        "s_add_u32 s24, s24, 128\n"
        "s_addc_u32 s25, s25, 0\n"
        "s_add_u32 s26, s26, 128\n"
        "s_addc_u32 s27, s27, 0\n"
        "s_add_u32 m0, s59, 67584\n"
        "s_mov_b32 exec_hi, 0\n"
        "global_load_lds_dwordx4 v37, s[26:27]\n"
        "s_mov_b32 exec_hi, -1\n"
        "v_lshl_add_u32 v54, v2, 12, v53\n"
        "s_add_u32 s51, s43, 0\n"
        "s_and_b32 s51, s51, 3\n"
        "s_lshl_b32 s51, s51, 16\n"
        "v_add_u32 v12, s51, v54\n"
        "v_add_u32 v13, 31744, v12\n"
        "s_add_u32 s51, s43, 1\n"
        "s_and_b32 s51, s51, 3\n"
        "s_lshl_b32 s51, s51, 16\n"
        "v_add_u32 v14, s51, v54\n"
        "v_add_u32 v15, 31744, v14\n"
        "s_add_u32 s51, s43, 2\n"
        "s_and_b32 s51, s51, 3\n"
        "s_lshl_b32 s51, s51, 16\n"
        "v_add_u32 v16, s51, v54\n"
        "v_add_u32 v17, 31744, v16\n"
        "s_add_u32 s51, s43, 3\n"
        "s_and_b32 s51, s51, 3\n"
        "s_lshl_b32 s51, s51, 16\n"
        "v_add_u32 v18, s51, v54\n"
        "v_add_u32 v19, 31744, v18\n"
        "s_lshl2_add_u32 s51, s45, s50\n"
        "s_lshl_b32 s51, s51, 7\n"
        "s_add_u32 s8, s51, 0\n"
        "s_and_b32 s8, s8, 0x380\n"
        "s_add_u32 s9, s51, 128\n"
        "s_and_b32 s9, s9, 0x380\n"
        "s_add_u32 s10, s51, 256\n"
        "s_and_b32 s10, s10, 0x380\n"
        "s_add_u32 s11, s51, 384\n"
        "s_and_b32 s11, s11, 0x380\n"
        "s_xor_b32 s12, s8, 512\n"
        "s_xor_b32 s13, s9, 512\n"
        "s_xor_b32 s14, s10, 512\n"
        "s_xor_b32 s15, s11, 512\n"
        "s_lshl_b32 s57, s46, 18\n"
        "s_lshl_b32 s52, s43, 10\n"
        "s_add_u32 s57, s57, s52\n"
        "s_cmp_eq_u32 s44, 0\n"
        "s_cselect_b64 s[28:29], s[66:67], s[68:69]\n"
        "s_add_u32 s16, s28, s57\n"
        "s_addc_u32 s17, s29, 0\n"
        "v_lshrrev_b32 v2, 4, v1\n"
        "v_and_b32 v53, 15, v1\n"
        "v_xor_b32 v54, v2, v3\n"
        "v_lshlrev_b32 v54, 4, v54\n"
        "v_lshl_add_u32 v54, v53, 7, v54\n"
        "v_add_u32 v28, s60, v54\n"
        "v_xor_b32 v29, 64, v28\n"
        "v_add_u32 v22, 16384, v54\n"
        "v_xor_b32 v23, 64, v22\n"
        "v_bfe_u32 v54, v1, 1, 3\n"
        "v_xor_b32 v54, v54, v2\n"
        "v_lshlrev_b32 v54, 4, v54\n"
        "v_bfe_u32 v55, v1, 2, 2\n"
        "v_lshl_add_u32 v54, v55, 10, v54\n"
        "v_and_b32 v55, 3, v1\n"
        "v_lshl_add_u32 v54, v55, 7, v54\n"
        "v_add_u32 v20, s58, v54\n"
        "v_xor_b32 v21, 64, v20\n"
        "v_add_u32 v24, 69632, v20\n"
        "v_add_u32 v25, 69632, v21\n"
        "v_add_u32 v26, 69632, v22\n"
        "v_add_u32 v27, 69632, v23\n"
        "v_lshlrev_b32 v44, 4, v1\n"
        "s_mul_i32 s51, s61, 3200\n"
        "s_add_u32 s51, s51, 0x490000\n"
        "s_add_u32 s38, s30, s51\n"
        "s_addc_u32 s39, s31, 0\n"
        "v_add_u32 v47, s60, v44\n"
        "s_sub_u32 s51, s43, 1\n"
        "s_and_b32 s51, s51, 3\n"
        "s_mul_i32 s51, s51, 14336\n"
        "s_add_u32 s51, s51, 104448\n"
        "v_add_u32 v48, s51, v44\n"
        "s_sub_u32 s51, s43, 2\n"
        "s_and_b32 s51, s51, 3\n"
        "s_mul_i32 s51, s51, 14336\n"
        "s_add_u32 s51, s51, 105472\n"
        "v_add_u32 v49, s51, v44\n"
        "s_sub_u32 s51, s43, 3\n"
        "s_and_b32 s51, s51, 3\n"
        "s_mul_i32 s51, s51, 14336\n"
        "s_add_u32 s51, s51, 106496\n"
        "v_add_u32 v50, s51, v44\n"
        "s_mul_i32 s51, s43, 18432\n"
        "v_mul_u32_u24 v52, 0x480, v53\n"
        "v_lshl_add_u32 v52, v2, 4, v52\n"
        "v_add_u32 v52, s51, v52\n"
        "s_mul_i32 s51, s46, 73728\n"
        "s_mul_i32 s52, s45, 288\n"
        "s_add_u32 s51, s51, s52\n"
        "s_add_u32 s34, s74, s51\n"
        "s_addc_u32 s35, s75, 0\n"
        "s_mul_i32 s51, s47, 590976\n"
        "s_lshl_b32 s52, s43, 14\n"
        "s_add_u32 s52, s52, s51\n"
        "s_cmp_eq_u32 s44, 0\n"
        "s_cselect_b32 s62, 328320, 0\n"
        "s_add_u32 s52, s52, s62\n"
        "s_add_u32 s78, s30, s52\n"
        "s_addc_u32 s79, s31, 0\n"
        "s_xor_b32 s51, s61, 16\n"
        "s_mul_i32 s51, s51, 3200\n"
        "s_add_u32 s51, s51, 0x490000\n"
        "s_add_u32 s40, s30, s51\n"
        "s_addc_u32 s41, s31, 0\n"
        "s_and_b32 s51, s49, 3\n"
        "s_lshl2_add_u32 s51, s44, s51\n"
        "s_lshl_b32 s51, s51, 9\n"
        "s_add_u32 s51, s51, 0x7b0000\n"
        "v_bfe_u32 v54, v1, 3, 2\n"
        "v_lshlrev_b32 v54, 7, v54\n"
        "v_lshl_add_u32 v54, v3, 2, v54\n"
        "v_add_u32 v32, s51, v54\n"
        "s_and_b32 s51, s45, 1\n"
        "s_lshl2_add_u32 s51, s44, s51\n"
        "s_lshl_b32 s51, s51, 9\n"
        "s_lshr_b32 s52, s45, 1\n"
        "s_lshl2_add_u32 s51, s52, s51\n"
        "s_add_u32 s51, s51, 0x7b0000\n"
        "v_bfe_u32 v54, v1, 2, 2\n"
        "v_lshlrev_b32 v54, 7, v54\n"
        "v_and_b32 v55, 3, v1\n"
        "v_lshl_add_u32 v54, v55, 3, v54\n"
        "v_add_u32 v54, s51, v54\n"
        "v_cmp_gt_u32 vcc, 32, v1\n"
        "s_nop 1\n"
        "v_cndmask_b32 v32, v54, v32, vcc\n"
        "v_xor_b32 v54, 0xc00, v35\n"
        "v_cmp_gt_u32 vcc, 48, v1\n"
        "s_nop 1\n"
        "v_cndmask_b32 v32, v54, v32, vcc\n"
        "v_lshl_add_u32 v34, v1, 2, s19\n"
        "s_xor_b32 s51, s18, 192\n"
        "s_lshl_b32 s51, s51, 10\n"
        "s_add_u32 s51, s51, 0x7c0280\n"
        "v_writelane_b32 v34, s51, 48\n"
        "s_mov_b32 s22, -1\n"
        "s_mov_b32 s23, 0x5ffff\n"
        "v_readfirstlane_b32 s52, v35\n"
        "s_xor_b32 s51, s18, 64\n"
        "s_lshl_b32 s51, s51, 10\n"
        "s_add_u32 s51, s51, 0x7c0300\n"
        "v_writelane_b32 v34, s51, 50\n"
        "s_xor_b32 s52, s52, 0x400\n"
        "v_writelane_b32 v32, s52, 50\n"
        "s_lshl2_add_u32 s51, s44, s49\n"
        "s_mul_i32 s51, s51, 590976\n"
        "s_lshl2_add_u32 s52, s21, s20\n"
        "s_lshl2_add_u32 s52, s52, s43\n"
        "s_lshl_b32 s52, s52, 10\n"
        "s_add_u32 s51, s51, s52\n"
        "s_add_u32 s36, s30, s51\n"
        "s_addc_u32 s37, s31, 0\n"
        "s_waitcnt vmcnt(10)\n"
        "v_readlane_b32 s48, v31, 17\n"
        "s_lshl3_add_u32 s51, s43, 161792\n"
        "v_mov_b32 v39, s51\n"
        "v_mov_b32 v36, 161792\n"
        "s_add_u32 s48, s48, 1\n"
        "v_mov_b32 v54, s48\n"
        "s_cmp_eq_u32 s43, 0\n"
        "s_cbranch_scc0 Lw_1\n"
        "s_mov_b64 exec, 1\n"
        "global_store_dword v35, v54, s[30:31] sc1\n"
        "s_mov_b64 exec, -1\n"
        "Lw_1:\n"
        "v_cmp_eq_u32 vcc, s48, v31\n"
        "v_mov_b32 v42, s48\n"
        "s_nop 3\n"
        "s_cmp_lg_u64 vcc, 0\n"
        "s_cselect_b32 s51, 1, 0\n"
        "v_mov_b32 v43, s51\n"
        "ds_write_b64 v39, v[42:43]\n"
        "s_waitcnt lgkmcnt(0)\n"
        "s_add_u32 m0, s58, 34816\n"
        "s_nop 0\n"
        "global_load_lds_dwordx4 v4, s[24:25]\n"
        "global_load_lds_dwordx4 v5, s[24:25] offset:1024\n"
        "global_load_lds_dwordx4 v6, s[24:25] offset:2048\n"
        "global_load_lds_dwordx4 v7, s[24:25] offset:3072\n"
        "s_add_u32 m0, s58, 51200\n"
        "s_nop 0\n"
        "global_load_lds_dwordx4 v8, s[26:27]\n"
        "global_load_lds_dwordx4 v9, s[26:27] offset:1024\n"
        "global_load_lds_dwordx4 v10, s[26:27] offset:2048\n"
        "global_load_lds_dwordx4 v11, s[26:27] offset:3072\n"
        "s_add_u32 s24, s24, 128\n"
        "s_addc_u32 s25, s25, 0\n"
        "s_add_u32 s26, s26, 128\n"
        "s_addc_u32 s27, s27, 0\n"
        "s_waitcnt vmcnt(9)\n"
        "s_barrier\n"
        "ds_read_b128 v[224:227], v36\n"
        "ds_read_b128 v[228:231], v36 offset:16\n"
        "ds_read_b128 v[128:131], v20 offset:0\n"
        "ds_read_b128 v[132:135], v20 offset:512\n"
        "ds_read_b128 v[136:139], v22 offset:0\n"
        "ds_read_b128 v[140:143], v22 offset:2048\n"
        "ds_read_b128 v[144:147], v22 offset:4096\n"
        "ds_read_b128 v[148:151], v22 offset:6144\n"
        "ds_read_b128 v[152:155], v22 offset:8192\n"
        "ds_read_b128 v[156:159], v22 offset:10240\n"
        "ds_read_b128 v[160:163], v22 offset:12288\n"
        "ds_read_b128 v[164:167], v22 offset:14336\n"
        "ds_read_b128 v[168:171], v22 offset:16384\n"
        "s_waitcnt lgkmcnt(11)\n"
        "Ln1_2:\n"
        "v_xor_b32 v226, v224, v226\n"
        "v_xor_b32 v228, v224, v228\n"
        "v_xor_b32 v230, v224, v230\n"
        "v_or3_b32 v42, v225, v226, v227\n"
        "v_or3_b32 v42, v42, v228, v229\n"
        "v_or3_b32 v42, v42, v230, v231\n"
        "v_cmp_ne_u32 vcc, 0, v42\n"
        "s_nop 4\n"
        "s_cbranch_vccz Ln1ok_3\n"
        "v_readfirstlane_b32 s48, v224\n"
        "s_barrier\n"
        "s_nop 1\n"
        "s_add_u32 s48, s48, 1\n"
        "v_mov_b32 v54, s48\n"
        "s_cmp_eq_u32 s43, 0\n"
        "s_cbranch_scc0 Lw_4\n"
        "s_mov_b64 exec, 1\n"
        "global_store_dword v35, v54, s[30:31] sc1\n"
        "s_mov_b64 exec, -1\n"
        "Lw_4:\n"
        "v_cmp_eq_u32 vcc, s48, v31\n"
        "v_mov_b32 v42, s48\n"
        "s_nop 3\n"
        "s_cmp_lg_u64 vcc, 0\n"
        "s_cselect_b32 s51, 1, 0\n"
        "v_mov_b32 v43, s51\n"
        "ds_write_b64 v39, v[42:43]\n"
        "s_waitcnt lgkmcnt(0)\n"
        "s_barrier\n"
        "ds_read_b128 v[224:227], v36\n"
        "ds_read_b128 v[228:231], v36 offset:16\n"
        "s_waitcnt lgkmcnt(0)\n"
        "s_branch Ln1_2\n"
        "Ln1ok_3:\n"
        "s_waitcnt lgkmcnt(0)\n"
        "v_mfma_f32_16x16x32_bf16 v[56:59], v[128:131], v[136:139], 0\n"
        "ds_read_b128 v[172:175], v21 offset:0\n"
        "s_add_u32 m0, s59, 102400\n"
        "s_mov_b32 exec_hi, 0\n"
        "global_load_lds_dwordx4 v37, s[26:27]\n"
        "s_mov_b32 exec_hi, -1\n"
        "v_mfma_f32_16x16x32_bf16 v[60:63], v[132:135], v[136:139], 0\n"
        "ds_read_b128 v[176:179], v21 offset:512\n"
        "s_add_u32 m0, s58, 69632\n"
        "v_mfma_f32_16x16x32_bf16 v[64:67], v[128:131], v[140:143], 0\n"
        "ds_read_b128 v[180:183], v23 offset:0\n"
        "global_load_lds_dwordx4 v4, s[24:25]\n"
        "v_mfma_f32_16x16x32_bf16 v[68:71], v[132:135], v[140:143], 0\n"
        "ds_read_b128 v[184:187], v23 offset:2048\n"
        "v_mfma_f32_16x16x32_bf16 v[72:75], v[128:131], v[144:147], 0\n"
        "ds_read_b128 v[188:191], v23 offset:4096\n"
        "global_load_lds_dwordx4 v5, s[24:25] offset:1024\n"
        "v_mfma_f32_16x16x32_bf16 v[76:79], v[132:135], v[144:147], 0\n"
        "ds_read_b128 v[192:195], v23 offset:6144\n"
        "global_load_lds_dwordx4 v6, s[24:25] offset:2048\n"
        "v_mfma_f32_16x16x32_bf16 v[80:83], v[128:131], v[148:151], 0\n"
        "ds_read_b128 v[196:199], v23 offset:8192\n"
        "global_load_lds_dwordx4 v7, s[24:25] offset:3072\n"
        "v_mfma_f32_16x16x32_bf16 v[84:87], v[132:135], v[148:151], 0\n"
        "ds_read_b128 v[200:203], v23 offset:10240\n"
        "v_mfma_f32_16x16x32_bf16 v[88:91], v[128:131], v[152:155], 0\n"
        "ds_read_b128 v[204:207], v23 offset:12288\n"
        "s_add_u32 m0, s58, 86016\n"
        "v_mfma_f32_16x16x32_bf16 v[92:95], v[132:135], v[152:155], 0\n"
        "ds_read_b128 v[208:211], v23 offset:14336\n"
        "global_load_lds_dwordx4 v8, s[26:27]\n"
        "v_mfma_f32_16x16x32_bf16 v[96:99], v[128:131], v[156:159], 0\n"
        "ds_read_b128 v[212:215], v23 offset:16384\n"
        "v_mfma_f32_16x16x32_bf16 v[100:103], v[132:135], v[156:159], 0\n"
        "global_load_lds_dwordx4 v9, s[26:27] offset:1024\n"
        "v_mfma_f32_16x16x32_bf16 v[104:107], v[128:131], v[160:163], 0\n"
        "global_load_lds_dwordx4 v10, s[26:27] offset:2048\n"
        "v_mfma_f32_16x16x32_bf16 v[108:111], v[132:135], v[160:163], 0\n"
        "global_load_lds_dwordx4 v11, s[26:27] offset:3072\n"
        "v_mfma_f32_16x16x32_bf16 v[112:115], v[128:131], v[164:167], 0\n"
        "v_mfma_f32_16x16x32_bf16 v[116:119], v[132:135], v[164:167], 0\n"
        "s_add_u32 s24, s24, 128\n"
        "s_addc_u32 s25, s25, 0\n"
        "v_mfma_f32_16x16x32_bf16 v[120:123], v[128:131], v[168:171], 0\n"
        "s_add_u32 s26, s26, 128\n"
        "s_addc_u32 s27, s27, 0\n"
        "v_mfma_f32_16x16x32_bf16 v[124:127], v[132:135], v[168:171], 0\n"
        "s_waitcnt vmcnt(9)\n"
        "s_waitcnt lgkmcnt(0)\n"
        "s_barrier\n"
        "v_mfma_f32_16x16x32_bf16 v[56:59], v[172:175], v[180:183], v[56:59]\n"
        "ds_read_b128 v[128:131], v20 offset:34816\n"
        "s_add_u32 m0, s59, 32768\n"
        "s_mov_b32 exec_hi, 0\n"
        "global_load_lds_dwordx4 v37, s[26:27]\n"
        "s_mov_b32 exec_hi, -1\n"
        "v_mfma_f32_16x16x32_bf16 v[60:63], v[176:179], v[180:183], v[60:63]\n"
        "ds_read_b128 v[132:135], v20 offset:35328\n"
        "v_mfma_f32_16x16x32_bf16 v[64:67], v[172:175], v[184:187], v[64:67]\n"
        "ds_read_b128 v[136:139], v22 offset:34816\n"
        "v_mfma_f32_16x16x32_bf16 v[68:71], v[176:179], v[184:187], v[68:71]\n"
        "ds_read_b128 v[140:143], v22 offset:36864\n"
        "s_add_u32 m0, s58, 0\n"
        "v_mfma_f32_16x16x32_bf16 v[72:75], v[172:175], v[188:191], v[72:75]\n"
        "ds_read_b128 v[144:147], v22 offset:38912\n"
        "v_mfma_f32_16x16x32_bf16 v[76:79], v[176:179], v[188:191], v[76:79]\n"
        "ds_read_b128 v[148:151], v22 offset:40960\n"
        "v_mfma_f32_16x16x32_bf16 v[80:83], v[172:175], v[192:195], v[80:83]\n"
        "ds_read_b128 v[152:155], v22 offset:43008\n"
        "global_load_lds_dwordx4 v4, s[24:25]\n"
        "v_mfma_f32_16x16x32_bf16 v[84:87], v[176:179], v[192:195], v[84:87]\n"
        "ds_read_b128 v[156:159], v22 offset:45056\n"
        "v_mfma_f32_16x16x32_bf16 v[88:91], v[172:175], v[196:199], v[88:91]\n"
        "ds_read_b128 v[160:163], v22 offset:47104\n"
        "v_mfma_f32_16x16x32_bf16 v[92:95], v[176:179], v[196:199], v[92:95]\n"
        "ds_read_b128 v[164:167], v22 offset:49152\n"
        "global_load_lds_dwordx4 v5, s[24:25] offset:1024\n"
        "v_mfma_f32_16x16x32_bf16 v[96:99], v[172:175], v[200:203], v[96:99]\n"
        "ds_read_b128 v[168:171], v22 offset:51200\n"
        "v_mfma_f32_16x16x32_bf16 v[100:103], v[176:179], v[200:203], v[100:103]\n"
        "v_mfma_f32_16x16x32_bf16 v[104:107], v[172:175], v[204:207], v[104:107]\n"
        "global_load_lds_dwordx4 v6, s[24:25] offset:2048\n"
        "v_mfma_f32_16x16x32_bf16 v[108:111], v[176:179], v[204:207], v[108:111]\n"
        "v_mfma_f32_16x16x32_bf16 v[112:115], v[172:175], v[208:211], v[112:115]\n"
        "v_mfma_f32_16x16x32_bf16 v[116:119], v[176:179], v[208:211], v[116:119]\n"
        "global_load_lds_dwordx4 v7, s[24:25] offset:3072\n"
        "v_mfma_f32_16x16x32_bf16 v[120:123], v[172:175], v[212:215], v[120:123]\n"
        "v_mfma_f32_16x16x32_bf16 v[124:127], v[176:179], v[212:215], v[124:127]\n"
        "s_waitcnt lgkmcnt(0)\n"
        "v_mfma_f32_16x16x32_bf16 v[56:59], v[128:131], v[136:139], v[56:59]\n"
        "ds_read_b128 v[172:175], v21 offset:34816\n"
        "s_add_u32 m0, s58, 16384\n"
        "v_mfma_f32_16x16x32_bf16 v[60:63], v[132:135], v[136:139], v[60:63]\n"
        "ds_read_b128 v[176:179], v21 offset:35328\n"
        "v_mfma_f32_16x16x32_bf16 v[64:67], v[128:131], v[140:143], v[64:67]\n"
        "ds_read_b128 v[180:183], v23 offset:34816\n"
        "global_load_lds_dwordx4 v8, s[26:27]\n"
        "v_mfma_f32_16x16x32_bf16 v[68:71], v[132:135], v[140:143], v[68:71]\n"
        "ds_read_b128 v[184:187], v23 offset:36864\n"
        "v_mfma_f32_16x16x32_bf16 v[72:75], v[128:131], v[144:147], v[72:75]\n"
        "ds_read_b128 v[188:191], v23 offset:38912\n"
        "v_mfma_f32_16x16x32_bf16 v[76:79], v[132:135], v[144:147], v[76:79]\n"
        "ds_read_b128 v[192:195], v23 offset:40960\n"
        "global_load_lds_dwordx4 v9, s[26:27] offset:1024\n"
        "v_mfma_f32_16x16x32_bf16 v[80:83], v[128:131], v[148:151], v[80:83]\n"
        "ds_read_b128 v[196:199], v23 offset:43008\n"
        "v_mfma_f32_16x16x32_bf16 v[84:87], v[132:135], v[148:151], v[84:87]\n"
        "ds_read_b128 v[200:203], v23 offset:45056\n"
        "global_load_lds_dwordx4 v10, s[26:27] offset:2048\n"
        "v_mfma_f32_16x16x32_bf16 v[88:91], v[128:131], v[152:155], v[88:91]\n"
        "ds_read_b128 v[204:207], v23 offset:47104\n"
        "v_mfma_f32_16x16x32_bf16 v[92:95], v[132:135], v[152:155], v[92:95]\n"
        "ds_read_b128 v[208:211], v23 offset:49152\n"
        "v_mfma_f32_16x16x32_bf16 v[96:99], v[128:131], v[156:159], v[96:99]\n"
        "ds_read_b128 v[212:215], v23 offset:51200\n"
        "global_load_lds_dwordx4 v11, s[26:27] offset:3072\n"
        "v_mfma_f32_16x16x32_bf16 v[100:103], v[132:135], v[156:159], v[100:103]\n"
        "v_mfma_f32_16x16x32_bf16 v[104:107], v[128:131], v[160:163], v[104:107]\n"
        "s_add_u32 s24, s24, 128\n"
        "s_addc_u32 s25, s25, 0\n"
        "v_mfma_f32_16x16x32_bf16 v[108:111], v[132:135], v[160:163], v[108:111]\n"
        "v_mfma_f32_16x16x32_bf16 v[112:115], v[128:131], v[164:167], v[112:115]\n"
        "v_mfma_f32_16x16x32_bf16 v[116:119], v[132:135], v[164:167], v[116:119]\n"
        "s_add_u32 s26, s26, 128\n"
        "s_addc_u32 s27, s27, 0\n"
        "v_mfma_f32_16x16x32_bf16 v[120:123], v[128:131], v[168:171], v[120:123]\n"
        "v_mfma_f32_16x16x32_bf16 v[124:127], v[132:135], v[168:171], v[124:127]\n"
        "s_waitcnt vmcnt(9)\n"
        "s_waitcnt lgkmcnt(0)\n"
        "s_barrier\n"
        "v_mfma_f32_16x16x32_bf16 v[56:59], v[172:175], v[180:183], v[56:59]\n"
        "ds_read_b128 v[128:131], v24 offset:0\n"
        "s_add_u32 m0, s59, 67584\n"
        "s_mov_b32 exec_hi, 0\n"
        "global_load_lds_dwordx4 v37, s[26:27]\n"
        "s_mov_b32 exec_hi, -1\n"
        "v_mfma_f32_16x16x32_bf16 v[60:63], v[176:179], v[180:183], v[60:63]\n"
        "ds_read_b128 v[132:135], v24 offset:512\n"
        "v_mfma_f32_16x16x32_bf16 v[64:67], v[172:175], v[184:187], v[64:67]\n"
        "ds_read_b128 v[136:139], v26 offset:0\n"
        "v_mfma_f32_16x16x32_bf16 v[68:71], v[176:179], v[184:187], v[68:71]\n"
        "ds_read_b128 v[140:143], v26 offset:2048\n"
        "s_add_u32 m0, s58, 34816\n"
        "v_mfma_f32_16x16x32_bf16 v[72:75], v[172:175], v[188:191], v[72:75]\n"
        "ds_read_b128 v[144:147], v26 offset:4096\n"
        "v_mfma_f32_16x16x32_bf16 v[76:79], v[176:179], v[188:191], v[76:79]\n"
        "ds_read_b128 v[148:151], v26 offset:6144\n"
        "v_mfma_f32_16x16x32_bf16 v[80:83], v[172:175], v[192:195], v[80:83]\n"
        "ds_read_b128 v[152:155], v26 offset:8192\n"
        "global_load_lds_dwordx4 v4, s[24:25]\n"
        "v_mfma_f32_16x16x32_bf16 v[84:87], v[176:179], v[192:195], v[84:87]\n"
        "ds_read_b128 v[156:159], v26 offset:10240\n"
        "v_mfma_f32_16x16x32_bf16 v[88:91], v[172:175], v[196:199], v[88:91]\n"
        "ds_read_b128 v[160:163], v26 offset:12288\n"
        "v_mfma_f32_16x16x32_bf16 v[92:95], v[176:179], v[196:199], v[92:95]\n"
        "ds_read_b128 v[164:167], v26 offset:14336\n"
        "global_load_lds_dwordx4 v5, s[24:25] offset:1024\n"
        "v_mfma_f32_16x16x32_bf16 v[96:99], v[172:175], v[200:203], v[96:99]\n"
        "ds_read_b128 v[168:171], v26 offset:16384\n"
        "v_mfma_f32_16x16x32_bf16 v[100:103], v[176:179], v[200:203], v[100:103]\n"
        "v_mfma_f32_16x16x32_bf16 v[104:107], v[172:175], v[204:207], v[104:107]\n"
        "global_load_lds_dwordx4 v6, s[24:25] offset:2048\n"
        "v_mfma_f32_16x16x32_bf16 v[108:111], v[176:179], v[204:207], v[108:111]\n"
        "v_mfma_f32_16x16x32_bf16 v[112:115], v[172:175], v[208:211], v[112:115]\n"
        "v_mfma_f32_16x16x32_bf16 v[116:119], v[176:179], v[208:211], v[116:119]\n"
        "global_load_lds_dwordx4 v7, s[24:25] offset:3072\n"
        "v_mfma_f32_16x16x32_bf16 v[120:123], v[172:175], v[212:215], v[120:123]\n"
        "v_mfma_f32_16x16x32_bf16 v[124:127], v[176:179], v[212:215], v[124:127]\n"
        "s_waitcnt lgkmcnt(0)\n"
        "v_mfma_f32_16x16x32_bf16 v[56:59], v[128:131], v[136:139], v[56:59]\n"
        "ds_read_b128 v[172:175], v25 offset:0\n"
        "s_add_u32 m0, s58, 51200\n"
        "v_mfma_f32_16x16x32_bf16 v[60:63], v[132:135], v[136:139], v[60:63]\n"
        "ds_read_b128 v[176:179], v25 offset:512\n"
        "v_mfma_f32_16x16x32_bf16 v[64:67], v[128:131], v[140:143], v[64:67]\n"
        "ds_read_b128 v[180:183], v27 offset:0\n"
        "global_load_lds_dwordx4 v8, s[26:27]\n"
        "v_mfma_f32_16x16x32_bf16 v[68:71], v[132:135], v[140:143], v[68:71]\n"
        "ds_read_b128 v[184:187], v27 offset:2048\n"
        "v_mfma_f32_16x16x32_bf16 v[72:75], v[128:131], v[144:147], v[72:75]\n"
        "ds_read_b128 v[188:191], v27 offset:4096\n"
        "v_mfma_f32_16x16x32_bf16 v[76:79], v[132:135], v[144:147], v[76:79]\n"
        "ds_read_b128 v[192:195], v27 offset:6144\n"
        "global_load_lds_dwordx4 v9, s[26:27] offset:1024\n"
        "v_mfma_f32_16x16x32_bf16 v[80:83], v[128:131], v[148:151], v[80:83]\n"
        "ds_read_b128 v[196:199], v27 offset:8192\n"
        "v_mfma_f32_16x16x32_bf16 v[84:87], v[132:135], v[148:151], v[84:87]\n"
        "ds_read_b128 v[200:203], v27 offset:10240\n"
        "global_load_lds_dwordx4 v10, s[26:27] offset:2048\n"
        "v_mfma_f32_16x16x32_bf16 v[88:91], v[128:131], v[152:155], v[88:91]\n"
        "ds_read_b128 v[204:207], v27 offset:12288\n"
        "v_mfma_f32_16x16x32_bf16 v[92:95], v[132:135], v[152:155], v[92:95]\n"
        "ds_read_b128 v[208:211], v27 offset:14336\n"
        "v_mfma_f32_16x16x32_bf16 v[96:99], v[128:131], v[156:159], v[96:99]\n"
        "ds_read_b128 v[212:215], v27 offset:16384\n"
        "global_load_lds_dwordx4 v11, s[26:27] offset:3072\n"
        "v_mfma_f32_16x16x32_bf16 v[100:103], v[132:135], v[156:159], v[100:103]\n"
        "v_mfma_f32_16x16x32_bf16 v[104:107], v[128:131], v[160:163], v[104:107]\n"
        "s_add_u32 s24, s24, 128\n"
        "s_addc_u32 s25, s25, 0\n"
        "v_mfma_f32_16x16x32_bf16 v[108:111], v[132:135], v[160:163], v[108:111]\n"
        "v_mfma_f32_16x16x32_bf16 v[112:115], v[128:131], v[164:167], v[112:115]\n"
        "v_mfma_f32_16x16x32_bf16 v[116:119], v[132:135], v[164:167], v[116:119]\n"
        "s_add_u32 s26, s26, 128\n"
        "s_addc_u32 s27, s27, 0\n"
        "v_mfma_f32_16x16x32_bf16 v[120:123], v[128:131], v[168:171], v[120:123]\n"
        "v_mfma_f32_16x16x32_bf16 v[124:127], v[132:135], v[168:171], v[124:127]\n"
        "s_waitcnt vmcnt(9)\n"
        "s_waitcnt lgkmcnt(0)\n"
        "s_barrier\n"
        "v_mfma_f32_16x16x32_bf16 v[56:59], v[172:175], v[180:183], v[56:59]\n"
        "ds_read_b128 v[128:131], v20 offset:0\n"
        "s_add_u32 m0, s59, 102400\n"
        "s_mov_b32 exec_hi, 0\n"
        "global_load_lds_dwordx4 v37, s[26:27]\n"
        "s_mov_b32 exec_hi, -1\n"
        "v_mfma_f32_16x16x32_bf16 v[60:63], v[176:179], v[180:183], v[60:63]\n"
        "ds_read_b128 v[132:135], v20 offset:512\n"
        "v_mfma_f32_16x16x32_bf16 v[64:67], v[172:175], v[184:187], v[64:67]\n"
        "ds_read_b128 v[136:139], v22 offset:0\n"
        "v_mfma_f32_16x16x32_bf16 v[68:71], v[176:179], v[184:187], v[68:71]\n"
        "ds_read_b128 v[140:143], v22 offset:2048\n"
        "s_add_u32 m0, s58, 69632\n"
        "v_mfma_f32_16x16x32_bf16 v[72:75], v[172:175], v[188:191], v[72:75]\n"
        "ds_read_b128 v[144:147], v22 offset:4096\n"
        "v_mfma_f32_16x16x32_bf16 v[76:79], v[176:179], v[188:191], v[76:79]\n"
        "ds_read_b128 v[148:151], v22 offset:6144\n"
        "v_mfma_f32_16x16x32_bf16 v[80:83], v[172:175], v[192:195], v[80:83]\n"
        "ds_read_b128 v[152:155], v22 offset:8192\n"
        "global_load_lds_dwordx4 v4, s[24:25]\n"
        "v_mfma_f32_16x16x32_bf16 v[84:87], v[176:179], v[192:195], v[84:87]\n"
        "ds_read_b128 v[156:159], v22 offset:10240\n"
        "v_mfma_f32_16x16x32_bf16 v[88:91], v[172:175], v[196:199], v[88:91]\n"
        "ds_read_b128 v[160:163], v22 offset:12288\n"
        "v_mfma_f32_16x16x32_bf16 v[92:95], v[176:179], v[196:199], v[92:95]\n"
        "ds_read_b128 v[164:167], v22 offset:14336\n"
        "global_load_lds_dwordx4 v5, s[24:25] offset:1024\n"
        "v_mfma_f32_16x16x32_bf16 v[96:99], v[172:175], v[200:203], v[96:99]\n"
        "ds_read_b128 v[168:171], v22 offset:16384\n"
        "v_mfma_f32_16x16x32_bf16 v[100:103], v[176:179], v[200:203], v[100:103]\n"
        "v_mfma_f32_16x16x32_bf16 v[104:107], v[172:175], v[204:207], v[104:107]\n"
        "global_load_lds_dwordx4 v6, s[24:25] offset:2048\n"
        "v_mfma_f32_16x16x32_bf16 v[108:111], v[176:179], v[204:207], v[108:111]\n"
        "v_mfma_f32_16x16x32_bf16 v[112:115], v[172:175], v[208:211], v[112:115]\n"
        "v_mfma_f32_16x16x32_bf16 v[116:119], v[176:179], v[208:211], v[116:119]\n"
        "global_load_lds_dwordx4 v7, s[24:25] offset:3072\n"
        "v_mfma_f32_16x16x32_bf16 v[120:123], v[172:175], v[212:215], v[120:123]\n"
        "v_mfma_f32_16x16x32_bf16 v[124:127], v[176:179], v[212:215], v[124:127]\n"
        "s_waitcnt lgkmcnt(0)\n"
        "v_mfma_f32_16x16x32_bf16 v[56:59], v[128:131], v[136:139], v[56:59]\n"
        "ds_read_b128 v[172:175], v21 offset:0\n"
        "s_add_u32 m0, s58, 86016\n"
        "v_mfma_f32_16x16x32_bf16 v[60:63], v[132:135], v[136:139], v[60:63]\n"
        "ds_read_b128 v[176:179], v21 offset:512\n"
        "v_mfma_f32_16x16x32_bf16 v[64:67], v[128:131], v[140:143], v[64:67]\n"
        "ds_read_b128 v[180:183], v23 offset:0\n"
        "global_load_lds_dwordx4 v8, s[26:27]\n"
        "v_mfma_f32_16x16x32_bf16 v[68:71], v[132:135], v[140:143], v[68:71]\n"
        "ds_read_b128 v[184:187], v23 offset:2048\n"
        "v_mfma_f32_16x16x32_bf16 v[72:75], v[128:131], v[144:147], v[72:75]\n"
        "ds_read_b128 v[188:191], v23 offset:4096\n"
        "v_mfma_f32_16x16x32_bf16 v[76:79], v[132:135], v[144:147], v[76:79]\n"
        "ds_read_b128 v[192:195], v23 offset:6144\n"
        "global_load_lds_dwordx4 v9, s[26:27] offset:1024\n"
        "v_mfma_f32_16x16x32_bf16 v[80:83], v[128:131], v[148:151], v[80:83]\n"
        "ds_read_b128 v[196:199], v23 offset:8192\n"
        "v_mfma_f32_16x16x32_bf16 v[84:87], v[132:135], v[148:151], v[84:87]\n"
        "ds_read_b128 v[200:203], v23 offset:10240\n"
        "global_load_lds_dwordx4 v10, s[26:27] offset:2048\n"
        "v_mfma_f32_16x16x32_bf16 v[88:91], v[128:131], v[152:155], v[88:91]\n"
        "ds_read_b128 v[204:207], v23 offset:12288\n"
        "v_mfma_f32_16x16x32_bf16 v[92:95], v[132:135], v[152:155], v[92:95]\n"
        "ds_read_b128 v[208:211], v23 offset:14336\n"
        "v_mfma_f32_16x16x32_bf16 v[96:99], v[128:131], v[156:159], v[96:99]\n"
        "ds_read_b128 v[212:215], v23 offset:16384\n"
        "global_load_lds_dwordx4 v11, s[26:27] offset:3072\n"
        "v_mfma_f32_16x16x32_bf16 v[100:103], v[132:135], v[156:159], v[100:103]\n"
        "v_mfma_f32_16x16x32_bf16 v[104:107], v[128:131], v[160:163], v[104:107]\n"
        "s_add_u32 s24, s24, 128\n"
        "s_addc_u32 s25, s25, 0\n"
        "v_mfma_f32_16x16x32_bf16 v[108:111], v[132:135], v[160:163], v[108:111]\n"
        "v_mfma_f32_16x16x32_bf16 v[112:115], v[128:131], v[164:167], v[112:115]\n"
        "v_mfma_f32_16x16x32_bf16 v[116:119], v[132:135], v[164:167], v[116:119]\n"
        "s_add_u32 s26, s26, 128\n"
        "s_addc_u32 s27, s27, 0\n"
        "v_mfma_f32_16x16x32_bf16 v[120:123], v[128:131], v[168:171], v[120:123]\n"
        "v_mfma_f32_16x16x32_bf16 v[124:127], v[132:135], v[168:171], v[124:127]\n"
        "s_waitcnt vmcnt(9)\n"
        "s_waitcnt lgkmcnt(0)\n"
        "s_barrier\n"
        "v_mfma_f32_16x16x32_bf16 v[56:59], v[172:175], v[180:183], v[56:59]\n"
        "ds_read_b128 v[128:131], v20 offset:34816\n"
        "s_add_u32 m0, s59, 32768\n"
        "s_mov_b32 exec_hi, 0\n"
        "global_load_lds_dwordx4 v37, s[26:27]\n"
        "s_mov_b32 exec_hi, -1\n"
        "v_mfma_f32_16x16x32_bf16 v[60:63], v[176:179], v[180:183], v[60:63]\n"
        "ds_read_b128 v[132:135], v20 offset:35328\n"
        "v_mfma_f32_16x16x32_bf16 v[64:67], v[172:175], v[184:187], v[64:67]\n"
        "ds_read_b128 v[136:139], v22 offset:34816\n"
        "v_mfma_f32_16x16x32_bf16 v[68:71], v[176:179], v[184:187], v[68:71]\n"
        "ds_read_b128 v[140:143], v22 offset:36864\n"
        "s_add_u32 m0, s58, 0\n"
        "v_mfma_f32_16x16x32_bf16 v[72:75], v[172:175], v[188:191], v[72:75]\n"
        "ds_read_b128 v[144:147], v22 offset:38912\n"
        "v_mfma_f32_16x16x32_bf16 v[76:79], v[176:179], v[188:191], v[76:79]\n"
        "ds_read_b128 v[148:151], v22 offset:40960\n"
        "v_mfma_f32_16x16x32_bf16 v[80:83], v[172:175], v[192:195], v[80:83]\n"
        "ds_read_b128 v[152:155], v22 offset:43008\n"
        "global_load_lds_dwordx4 v4, s[24:25]\n"
        "v_mfma_f32_16x16x32_bf16 v[84:87], v[176:179], v[192:195], v[84:87]\n"
        "ds_read_b128 v[156:159], v22 offset:45056\n"
        "v_mfma_f32_16x16x32_bf16 v[88:91], v[172:175], v[196:199], v[88:91]\n"
        "ds_read_b128 v[160:163], v22 offset:47104\n"
        "v_mfma_f32_16x16x32_bf16 v[92:95], v[176:179], v[196:199], v[92:95]\n"
        "ds_read_b128 v[164:167], v22 offset:49152\n"
        "global_load_lds_dwordx4 v5, s[24:25] offset:1024\n"
        "v_mfma_f32_16x16x32_bf16 v[96:99], v[172:175], v[200:203], v[96:99]\n"
        "ds_read_b128 v[168:171], v22 offset:51200\n"
        "v_mfma_f32_16x16x32_bf16 v[100:103], v[176:179], v[200:203], v[100:103]\n"
        "v_mfma_f32_16x16x32_bf16 v[104:107], v[172:175], v[204:207], v[104:107]\n"
        "global_load_lds_dwordx4 v6, s[24:25] offset:2048\n"
        "v_mfma_f32_16x16x32_bf16 v[108:111], v[176:179], v[204:207], v[108:111]\n"
        "v_mfma_f32_16x16x32_bf16 v[112:115], v[172:175], v[208:211], v[112:115]\n"
        "v_mfma_f32_16x16x32_bf16 v[116:119], v[176:179], v[208:211], v[116:119]\n"
        "global_load_lds_dwordx4 v7, s[24:25] offset:3072\n"
        "v_mfma_f32_16x16x32_bf16 v[120:123], v[172:175], v[212:215], v[120:123]\n"
        "v_mfma_f32_16x16x32_bf16 v[124:127], v[176:179], v[212:215], v[124:127]\n"
        "s_waitcnt lgkmcnt(0)\n"
        "v_mfma_f32_16x16x32_bf16 v[56:59], v[128:131], v[136:139], v[56:59]\n"
        "ds_read_b128 v[172:175], v21 offset:34816\n"
        "s_add_u32 m0, s58, 16384\n"
        "v_mfma_f32_16x16x32_bf16 v[60:63], v[132:135], v[136:139], v[60:63]\n"
        "ds_read_b128 v[176:179], v21 offset:35328\n"
        "v_mfma_f32_16x16x32_bf16 v[64:67], v[128:131], v[140:143], v[64:67]\n"
        "ds_read_b128 v[180:183], v23 offset:34816\n"
        "global_load_lds_dwordx4 v8, s[26:27]\n"
        "v_mfma_f32_16x16x32_bf16 v[68:71], v[132:135], v[140:143], v[68:71]\n"
        "ds_read_b128 v[184:187], v23 offset:36864\n"
        "v_mfma_f32_16x16x32_bf16 v[72:75], v[128:131], v[144:147], v[72:75]\n"
        "ds_read_b128 v[188:191], v23 offset:38912\n"
        "v_mfma_f32_16x16x32_bf16 v[76:79], v[132:135], v[144:147], v[76:79]\n"
        "ds_read_b128 v[192:195], v23 offset:40960\n"
        "global_load_lds_dwordx4 v9, s[26:27] offset:1024\n"
        "v_mfma_f32_16x16x32_bf16 v[80:83], v[128:131], v[148:151], v[80:83]\n"
        "ds_read_b128 v[196:199], v23 offset:43008\n"
        "v_mfma_f32_16x16x32_bf16 v[84:87], v[132:135], v[148:151], v[84:87]\n"
        "ds_read_b128 v[200:203], v23 offset:45056\n"
        "global_load_lds_dwordx4 v10, s[26:27] offset:2048\n"
        "v_mfma_f32_16x16x32_bf16 v[88:91], v[128:131], v[152:155], v[88:91]\n"
        "ds_read_b128 v[204:207], v23 offset:47104\n"
        "v_mfma_f32_16x16x32_bf16 v[92:95], v[132:135], v[152:155], v[92:95]\n"
        "ds_read_b128 v[208:211], v23 offset:49152\n"
        "v_mfma_f32_16x16x32_bf16 v[96:99], v[128:131], v[156:159], v[96:99]\n"
        "ds_read_b128 v[212:215], v23 offset:51200\n"
        "global_load_lds_dwordx4 v11, s[26:27] offset:3072\n"
        "v_mfma_f32_16x16x32_bf16 v[100:103], v[132:135], v[156:159], v[100:103]\n"
        "v_mfma_f32_16x16x32_bf16 v[104:107], v[128:131], v[160:163], v[104:107]\n"
        "s_add_u32 s24, s24, 128\n"
        "s_addc_u32 s25, s25, 0\n"
        "v_mfma_f32_16x16x32_bf16 v[108:111], v[132:135], v[160:163], v[108:111]\n"
        "v_mfma_f32_16x16x32_bf16 v[112:115], v[128:131], v[164:167], v[112:115]\n"
        "v_mfma_f32_16x16x32_bf16 v[116:119], v[132:135], v[164:167], v[116:119]\n"
        "s_add_u32 s26, s26, 128\n"
        "s_addc_u32 s27, s27, 0\n"
        "v_mfma_f32_16x16x32_bf16 v[120:123], v[128:131], v[168:171], v[120:123]\n"
        "v_mfma_f32_16x16x32_bf16 v[124:127], v[132:135], v[168:171], v[124:127]\n"
        "s_waitcnt vmcnt(9)\n"
        "s_waitcnt lgkmcnt(0)\n"
        "s_barrier\n"
        "v_mfma_f32_16x16x32_bf16 v[56:59], v[172:175], v[180:183], v[56:59]\n"
        "ds_read_b128 v[128:131], v24 offset:0\n"
        "s_add_u32 m0, s59, 67584\n"
        "s_mov_b32 exec_hi, 0\n"
        "global_load_lds_dwordx4 v37, s[26:27]\n"
        "s_mov_b32 exec_hi, -1\n"
        "v_mfma_f32_16x16x32_bf16 v[60:63], v[176:179], v[180:183], v[60:63]\n"
        "ds_read_b128 v[132:135], v24 offset:512\n"
        "v_mfma_f32_16x16x32_bf16 v[64:67], v[172:175], v[184:187], v[64:67]\n"
        "ds_read_b128 v[136:139], v26 offset:0\n"
        "v_mfma_f32_16x16x32_bf16 v[68:71], v[176:179], v[184:187], v[68:71]\n"
        "ds_read_b128 v[140:143], v26 offset:2048\n"
        "s_add_u32 m0, s58, 34816\n"
        "v_mfma_f32_16x16x32_bf16 v[72:75], v[172:175], v[188:191], v[72:75]\n"
        "ds_read_b128 v[144:147], v26 offset:4096\n"
        "v_mfma_f32_16x16x32_bf16 v[76:79], v[176:179], v[188:191], v[76:79]\n"
        "ds_read_b128 v[148:151], v26 offset:6144\n"
        "v_mfma_f32_16x16x32_bf16 v[80:83], v[172:175], v[192:195], v[80:83]\n"
        "ds_read_b128 v[152:155], v26 offset:8192\n"
        "global_load_lds_dwordx4 v4, s[24:25]\n"
        "v_mfma_f32_16x16x32_bf16 v[84:87], v[176:179], v[192:195], v[84:87]\n"
        "ds_read_b128 v[156:159], v26 offset:10240\n"
        "v_mfma_f32_16x16x32_bf16 v[88:91], v[172:175], v[196:199], v[88:91]\n"
        "ds_read_b128 v[160:163], v26 offset:12288\n"
        "v_mfma_f32_16x16x32_bf16 v[92:95], v[176:179], v[196:199], v[92:95]\n"
        "ds_read_b128 v[164:167], v26 offset:14336\n"
        "global_load_lds_dwordx4 v5, s[24:25] offset:1024\n"
        "v_mfma_f32_16x16x32_bf16 v[96:99], v[172:175], v[200:203], v[96:99]\n"
        "ds_read_b128 v[168:171], v26 offset:16384\n"
        "v_mfma_f32_16x16x32_bf16 v[100:103], v[176:179], v[200:203], v[100:103]\n"
        "v_mfma_f32_16x16x32_bf16 v[104:107], v[172:175], v[204:207], v[104:107]\n"
        "global_load_lds_dwordx4 v6, s[24:25] offset:2048\n"
        "v_mfma_f32_16x16x32_bf16 v[108:111], v[176:179], v[204:207], v[108:111]\n"
        "v_mfma_f32_16x16x32_bf16 v[112:115], v[172:175], v[208:211], v[112:115]\n"
        "v_mfma_f32_16x16x32_bf16 v[116:119], v[176:179], v[208:211], v[116:119]\n"
        "global_load_lds_dwordx4 v7, s[24:25] offset:3072\n"
        "v_mfma_f32_16x16x32_bf16 v[120:123], v[172:175], v[212:215], v[120:123]\n"
        "v_mfma_f32_16x16x32_bf16 v[124:127], v[176:179], v[212:215], v[124:127]\n"
        "s_waitcnt lgkmcnt(0)\n"
        "v_mfma_f32_16x16x32_bf16 v[56:59], v[128:131], v[136:139], v[56:59]\n"
        "ds_read_b128 v[172:175], v25 offset:0\n"
        "s_add_u32 m0, s58, 51200\n"
        "v_mfma_f32_16x16x32_bf16 v[60:63], v[132:135], v[136:139], v[60:63]\n"
        "ds_read_b128 v[176:179], v25 offset:512\n"
        "v_mfma_f32_16x16x32_bf16 v[64:67], v[128:131], v[140:143], v[64:67]\n"
        "ds_read_b128 v[180:183], v27 offset:0\n"
        "global_load_lds_dwordx4 v8, s[26:27]\n"
        "v_mfma_f32_16x16x32_bf16 v[68:71], v[132:135], v[140:143], v[68:71]\n"
        "ds_read_b128 v[184:187], v27 offset:2048\n"
        "v_mfma_f32_16x16x32_bf16 v[72:75], v[128:131], v[144:147], v[72:75]\n"
        "ds_read_b128 v[188:191], v27 offset:4096\n"
        "v_mfma_f32_16x16x32_bf16 v[76:79], v[132:135], v[144:147], v[76:79]\n"
        "ds_read_b128 v[192:195], v27 offset:6144\n"
        "global_load_lds_dwordx4 v9, s[26:27] offset:1024\n"
        "v_mfma_f32_16x16x32_bf16 v[80:83], v[128:131], v[148:151], v[80:83]\n"
        "ds_read_b128 v[196:199], v27 offset:8192\n"
        "v_mfma_f32_16x16x32_bf16 v[84:87], v[132:135], v[148:151], v[84:87]\n"
        "ds_read_b128 v[200:203], v27 offset:10240\n"
        "global_load_lds_dwordx4 v10, s[26:27] offset:2048\n"
        "v_mfma_f32_16x16x32_bf16 v[88:91], v[128:131], v[152:155], v[88:91]\n"
        "ds_read_b128 v[204:207], v27 offset:12288\n"
        "v_mfma_f32_16x16x32_bf16 v[92:95], v[132:135], v[152:155], v[92:95]\n"
        "ds_read_b128 v[208:211], v27 offset:14336\n"
        "v_mfma_f32_16x16x32_bf16 v[96:99], v[128:131], v[156:159], v[96:99]\n"
        "ds_read_b128 v[212:215], v27 offset:16384\n"
        "global_load_lds_dwordx4 v11, s[26:27] offset:3072\n"
        "v_mfma_f32_16x16x32_bf16 v[100:103], v[132:135], v[156:159], v[100:103]\n"
        "v_mfma_f32_16x16x32_bf16 v[104:107], v[128:131], v[160:163], v[104:107]\n"
        "s_add_u32 s24, s24, 128\n"
        "s_addc_u32 s25, s25, 0\n"
        "v_mfma_f32_16x16x32_bf16 v[108:111], v[132:135], v[160:163], v[108:111]\n"
        "v_mfma_f32_16x16x32_bf16 v[112:115], v[128:131], v[164:167], v[112:115]\n"
        "v_mfma_f32_16x16x32_bf16 v[116:119], v[132:135], v[164:167], v[116:119]\n"
        "s_add_u32 s26, s26, 128\n"
        "s_addc_u32 s27, s27, 0\n"
        "v_mfma_f32_16x16x32_bf16 v[120:123], v[128:131], v[168:171], v[120:123]\n"
        "v_mfma_f32_16x16x32_bf16 v[124:127], v[132:135], v[168:171], v[124:127]\n"
        "s_waitcnt vmcnt(9)\n"
        "s_waitcnt lgkmcnt(0)\n"
        "s_barrier\n"
        "v_mfma_f32_16x16x32_bf16 v[56:59], v[172:175], v[180:183], v[56:59]\n"
        "ds_read_b128 v[128:131], v20 offset:0\n"
        "s_add_u32 m0, s59, 102400\n"
        "s_mov_b32 exec_hi, 0\n"
        "global_load_lds_dwordx4 v37, s[26:27]\n"
        "s_mov_b32 exec_hi, -1\n"
        "v_mfma_f32_16x16x32_bf16 v[60:63], v[176:179], v[180:183], v[60:63]\n"
        "ds_read_b128 v[132:135], v20 offset:512\n"
        "v_mfma_f32_16x16x32_bf16 v[64:67], v[172:175], v[184:187], v[64:67]\n"
        "ds_read_b128 v[136:139], v22 offset:0\n"
        "v_mfma_f32_16x16x32_bf16 v[68:71], v[176:179], v[184:187], v[68:71]\n"
        "ds_read_b128 v[140:143], v22 offset:2048\n"
        "s_add_u32 m0, s58, 69632\n"
        "v_mfma_f32_16x16x32_bf16 v[72:75], v[172:175], v[188:191], v[72:75]\n"
        "ds_read_b128 v[144:147], v22 offset:4096\n"
        "v_mfma_f32_16x16x32_bf16 v[76:79], v[176:179], v[188:191], v[76:79]\n"
        "ds_read_b128 v[148:151], v22 offset:6144\n"
        "v_mfma_f32_16x16x32_bf16 v[80:83], v[172:175], v[192:195], v[80:83]\n"
        "ds_read_b128 v[152:155], v22 offset:8192\n"
        "global_load_lds_dwordx4 v4, s[24:25]\n"
        "v_mfma_f32_16x16x32_bf16 v[84:87], v[176:179], v[192:195], v[84:87]\n"
        "ds_read_b128 v[156:159], v22 offset:10240\n"
        "v_mfma_f32_16x16x32_bf16 v[88:91], v[172:175], v[196:199], v[88:91]\n"
        "ds_read_b128 v[160:163], v22 offset:12288\n"
        "v_mfma_f32_16x16x32_bf16 v[92:95], v[176:179], v[196:199], v[92:95]\n"
        "ds_read_b128 v[164:167], v22 offset:14336\n"
        "global_load_lds_dwordx4 v5, s[24:25] offset:1024\n"
        "v_mfma_f32_16x16x32_bf16 v[96:99], v[172:175], v[200:203], v[96:99]\n"
        "ds_read_b128 v[168:171], v22 offset:16384\n"
        "v_mfma_f32_16x16x32_bf16 v[100:103], v[176:179], v[200:203], v[100:103]\n"
        "v_mfma_f32_16x16x32_bf16 v[104:107], v[172:175], v[204:207], v[104:107]\n"
        "global_load_lds_dwordx4 v6, s[24:25] offset:2048\n"
        "v_mfma_f32_16x16x32_bf16 v[108:111], v[176:179], v[204:207], v[108:111]\n"
        "v_mfma_f32_16x16x32_bf16 v[112:115], v[172:175], v[208:211], v[112:115]\n"
        "v_mfma_f32_16x16x32_bf16 v[116:119], v[176:179], v[208:211], v[116:119]\n"
        "global_load_lds_dwordx4 v7, s[24:25] offset:3072\n"
        "v_mfma_f32_16x16x32_bf16 v[120:123], v[172:175], v[212:215], v[120:123]\n"
        "v_mfma_f32_16x16x32_bf16 v[124:127], v[176:179], v[212:215], v[124:127]\n"
        "s_waitcnt lgkmcnt(0)\n"
        "v_mfma_f32_16x16x32_bf16 v[56:59], v[128:131], v[136:139], v[56:59]\n"
        "ds_read_b128 v[172:175], v21 offset:0\n"
        "s_add_u32 m0, s58, 86016\n"
        "v_mfma_f32_16x16x32_bf16 v[60:63], v[132:135], v[136:139], v[60:63]\n"
        "ds_read_b128 v[176:179], v21 offset:512\n"
        "v_mfma_f32_16x16x32_bf16 v[64:67], v[128:131], v[140:143], v[64:67]\n"
        "ds_read_b128 v[180:183], v23 offset:0\n"
        "global_load_lds_dwordx4 v8, s[26:27]\n"
        "v_mfma_f32_16x16x32_bf16 v[68:71], v[132:135], v[140:143], v[68:71]\n"
        "ds_read_b128 v[184:187], v23 offset:2048\n"
        "v_mfma_f32_16x16x32_bf16 v[72:75], v[128:131], v[144:147], v[72:75]\n"
        "ds_read_b128 v[188:191], v23 offset:4096\n"
        "global_load_lds_dwordx4 v9, s[26:27] offset:1024\n"
        "v_mfma_f32_16x16x32_bf16 v[76:79], v[132:135], v[144:147], v[76:79]\n"
        "ds_read_b128 v[192:195], v23 offset:6144\n"
        "v_mfma_f32_16x16x32_bf16 v[80:83], v[128:131], v[148:151], v[80:83]\n"
        "ds_read_b128 v[196:199], v23 offset:8192\n"
        "global_load_lds_dwordx4 v10, s[26:27] offset:2048\n"
        "v_mfma_f32_16x16x32_bf16 v[84:87], v[132:135], v[148:151], v[84:87]\n"
        "ds_read_b128 v[200:203], v23 offset:10240\n"
        "v_mfma_f32_16x16x32_bf16 v[88:91], v[128:131], v[152:155], v[88:91]\n"
        "ds_read_b128 v[204:207], v23 offset:12288\n"
        "global_load_lds_dwordx4 v11, s[26:27] offset:3072\n"
        "v_mfma_f32_16x16x32_bf16 v[92:95], v[132:135], v[152:155], v[92:95]\n"
        "ds_read_b128 v[208:211], v23 offset:14336\n"
        "v_mfma_f32_16x16x32_bf16 v[96:99], v[128:131], v[156:159], v[96:99]\n"
        "ds_read_b128 v[212:215], v23 offset:16384\n"
        "s_add_u32 m0, s60, 0\n"
        "v_mfma_f32_16x16x32_bf16 v[100:103], v[132:135], v[156:159], v[100:103]\n"
        "v_mfma_f32_16x16x32_bf16 v[104:107], v[128:131], v[160:163], v[104:107]\n"
        "s_add_u32 s28, s16, s8\n"
        "s_addc_u32 s29, s17, 0\n"
        "v_mfma_f32_16x16x32_bf16 v[108:111], v[132:135], v[160:163], v[108:111]\n"
        "v_mfma_f32_16x16x32_bf16 v[112:115], v[128:131], v[164:167], v[112:115]\n"
        "global_load_lds_dwordx4 v12, s[28:29]\n"
        "v_mfma_f32_16x16x32_bf16 v[116:119], v[132:135], v[164:167], v[116:119]\n"
        "v_mfma_f32_16x16x32_bf16 v[120:123], v[128:131], v[168:171], v[120:123]\n"
        "global_load_lds_dwordx4 v13, s[28:29] offset:1024\n"
        "v_mfma_f32_16x16x32_bf16 v[124:127], v[132:135], v[168:171], v[124:127]\n"
        "s_waitcnt vmcnt(11)\n"
        "s_waitcnt lgkmcnt(0)\n"
        "s_barrier\n"
        "v_mfma_f32_16x16x32_bf16 v[56:59], v[172:175], v[180:183], v[56:59]\n"
        "ds_read_b128 v[128:131], v20 offset:34816\n"
        "global_load_dword v33, v32, s[30:31] sc1\n"
        "v_mfma_f32_16x16x32_bf16 v[60:63], v[176:179], v[180:183], v[60:63]\n"
        "ds_read_b128 v[132:135], v20 offset:35328\n"
        "v_mfma_f32_16x16x32_bf16 v[64:67], v[172:175], v[184:187], v[64:67]\n"
        "ds_read_b128 v[136:139], v22 offset:34816\n"
        "s_add_u32 m0, s60, 2048\n"
        "v_mfma_f32_16x16x32_bf16 v[68:71], v[176:179], v[184:187], v[68:71]\n"
        "ds_read_b128 v[140:143], v22 offset:36864\n"
        "v_mfma_f32_16x16x32_bf16 v[72:75], v[172:175], v[188:191], v[72:75]\n"
        "ds_read_b128 v[144:147], v22 offset:38912\n"
        "s_add_u32 s28, s16, s9\n"
        "s_addc_u32 s29, s17, 0\n"
        "v_mfma_f32_16x16x32_bf16 v[76:79], v[176:179], v[188:191], v[76:79]\n"
        "ds_read_b128 v[148:151], v22 offset:40960\n"
        "v_mfma_f32_16x16x32_bf16 v[80:83], v[172:175], v[192:195], v[80:83]\n"
        "ds_read_b128 v[152:155], v22 offset:43008\n"
        "global_load_lds_dwordx4 v12, s[28:29]\n"
        "v_mfma_f32_16x16x32_bf16 v[84:87], v[176:179], v[192:195], v[84:87]\n"
        "ds_read_b128 v[156:159], v22 offset:45056\n"
        "v_mfma_f32_16x16x32_bf16 v[88:91], v[172:175], v[196:199], v[88:91]\n"
        "ds_read_b128 v[160:163], v22 offset:47104\n"
        "global_load_lds_dwordx4 v13, s[28:29] offset:1024\n"
        "v_mfma_f32_16x16x32_bf16 v[92:95], v[176:179], v[196:199], v[92:95]\n"
        "ds_read_b128 v[164:167], v22 offset:49152\n"
        "v_mfma_f32_16x16x32_bf16 v[96:99], v[172:175], v[200:203], v[96:99]\n"
        "ds_read_b128 v[168:171], v22 offset:51200\n"
        "s_add_u32 m0, s60, 4096\n"
        "v_mfma_f32_16x16x32_bf16 v[100:103], v[176:179], v[200:203], v[100:103]\n"
        "v_mfma_f32_16x16x32_bf16 v[104:107], v[172:175], v[204:207], v[104:107]\n"
        "s_add_u32 s28, s16, s10\n"
        "s_addc_u32 s29, s17, 0\n"
        "v_mfma_f32_16x16x32_bf16 v[108:111], v[176:179], v[204:207], v[108:111]\n"
        "v_mfma_f32_16x16x32_bf16 v[112:115], v[172:175], v[208:211], v[112:115]\n"
        "global_load_lds_dwordx4 v12, s[28:29]\n"
        "v_mfma_f32_16x16x32_bf16 v[116:119], v[176:179], v[208:211], v[116:119]\n"
        "v_mfma_f32_16x16x32_bf16 v[120:123], v[172:175], v[212:215], v[120:123]\n"
        "global_load_lds_dwordx4 v13, s[28:29] offset:1024\n"
        "v_mfma_f32_16x16x32_bf16 v[124:127], v[176:179], v[212:215], v[124:127]\n"
        "s_waitcnt lgkmcnt(0)\n"
        "v_mfma_f32_16x16x32_bf16 v[56:59], v[128:131], v[136:139], v[56:59]\n"
        "ds_read_b128 v[172:175], v21 offset:34816\n"
        "s_add_u32 m0, s60, 6144\n"
        "v_mfma_f32_16x16x32_bf16 v[60:63], v[132:135], v[136:139], v[60:63]\n"
        "ds_read_b128 v[176:179], v21 offset:35328\n"
        "v_mfma_f32_16x16x32_bf16 v[64:67], v[128:131], v[140:143], v[64:67]\n"
        "ds_read_b128 v[180:183], v23 offset:34816\n"
        "s_add_u32 s28, s16, s11\n"
        "s_addc_u32 s29, s17, 0\n"
        "v_mfma_f32_16x16x32_bf16 v[68:71], v[132:135], v[140:143], v[68:71]\n"
        "ds_read_b128 v[184:187], v23 offset:36864\n"
        "v_mfma_f32_16x16x32_bf16 v[72:75], v[128:131], v[144:147], v[72:75]\n"
        "ds_read_b128 v[188:191], v23 offset:38912\n"
        "global_load_lds_dwordx4 v12, s[28:29]\n"
        "v_mfma_f32_16x16x32_bf16 v[76:79], v[132:135], v[144:147], v[76:79]\n"
        "ds_read_b128 v[192:195], v23 offset:40960\n"
        "v_mfma_f32_16x16x32_bf16 v[80:83], v[128:131], v[148:151], v[80:83]\n"
        "ds_read_b128 v[196:199], v23 offset:43008\n"
        "global_load_lds_dwordx4 v13, s[28:29] offset:1024\n"
        "v_mfma_f32_16x16x32_bf16 v[84:87], v[132:135], v[148:151], v[84:87]\n"
        "ds_read_b128 v[200:203], v23 offset:45056\n"
        "v_mfma_f32_16x16x32_bf16 v[88:91], v[128:131], v[152:155], v[88:91]\n"
        "ds_read_b128 v[204:207], v23 offset:47104\n"
        "v_mfma_f32_16x16x32_bf16 v[92:95], v[132:135], v[152:155], v[92:95]\n"
        "ds_read_b128 v[208:211], v23 offset:49152\n"
        "s_add_u32 m0, s60, 8192\n"
        "v_mfma_f32_16x16x32_bf16 v[96:99], v[128:131], v[156:159], v[96:99]\n"
        "ds_read_b128 v[212:215], v23 offset:51200\n"
        "v_mfma_f32_16x16x32_bf16 v[100:103], v[132:135], v[156:159], v[100:103]\n"
        "s_add_u32 s28, s16, s12\n"
        "s_addc_u32 s29, s17, 0\n"
        "v_mfma_f32_16x16x32_bf16 v[104:107], v[128:131], v[160:163], v[104:107]\n"
        "v_mfma_f32_16x16x32_bf16 v[108:111], v[132:135], v[160:163], v[108:111]\n"
        "global_load_lds_dwordx4 v12, s[28:29]\n"
        "v_mfma_f32_16x16x32_bf16 v[112:115], v[128:131], v[164:167], v[112:115]\n"
        "v_mfma_f32_16x16x32_bf16 v[116:119], v[132:135], v[164:167], v[116:119]\n"
        "global_load_lds_dwordx4 v13, s[28:29] offset:1024\n"
        "v_mfma_f32_16x16x32_bf16 v[120:123], v[128:131], v[168:171], v[120:123]\n"
        "v_mfma_f32_16x16x32_bf16 v[124:127], v[132:135], v[168:171], v[124:127]\n"
        "s_waitcnt vmcnt(11)\n"
        "s_waitcnt lgkmcnt(0)\n"
        "s_barrier\n"
        "v_mfma_f32_16x16x32_bf16 v[56:59], v[172:175], v[180:183], v[56:59]\n"
        "ds_read_b128 v[128:131], v24 offset:0\n"
        "s_add_u32 m0, s60, 10240\n"
        "v_mfma_f32_16x16x32_bf16 v[60:63], v[176:179], v[180:183], v[60:63]\n"
        "ds_read_b128 v[132:135], v24 offset:512\n"
        "v_mfma_f32_16x16x32_bf16 v[64:67], v[172:175], v[184:187], v[64:67]\n"
        "ds_read_b128 v[136:139], v26 offset:0\n"
        "s_add_u32 s28, s16, s13\n"
        "s_addc_u32 s29, s17, 0\n"
        "v_mfma_f32_16x16x32_bf16 v[68:71], v[176:179], v[184:187], v[68:71]\n"
        "ds_read_b128 v[140:143], v26 offset:2048\n"
        "v_mfma_f32_16x16x32_bf16 v[72:75], v[172:175], v[188:191], v[72:75]\n"
        "ds_read_b128 v[144:147], v26 offset:4096\n"
        "global_load_lds_dwordx4 v12, s[28:29]\n"
        "v_mfma_f32_16x16x32_bf16 v[76:79], v[176:179], v[188:191], v[76:79]\n"
        "ds_read_b128 v[148:151], v26 offset:6144\n"
        "v_mfma_f32_16x16x32_bf16 v[80:83], v[172:175], v[192:195], v[80:83]\n"
        "ds_read_b128 v[152:155], v26 offset:8192\n"
        "global_load_lds_dwordx4 v13, s[28:29] offset:1024\n"
        "v_mfma_f32_16x16x32_bf16 v[84:87], v[176:179], v[192:195], v[84:87]\n"
        "ds_read_b128 v[156:159], v26 offset:10240\n"
        "v_mfma_f32_16x16x32_bf16 v[88:91], v[172:175], v[196:199], v[88:91]\n"
        "ds_read_b128 v[160:163], v26 offset:12288\n"
        "s_add_u32 m0, s60, 12288\n"
        "v_mfma_f32_16x16x32_bf16 v[92:95], v[176:179], v[196:199], v[92:95]\n"
        "ds_read_b128 v[164:167], v26 offset:14336\n"
        "v_mfma_f32_16x16x32_bf16 v[96:99], v[172:175], v[200:203], v[96:99]\n"
        "ds_read_b128 v[168:171], v26 offset:16384\n"
        "s_add_u32 s28, s16, s14\n"
        "s_addc_u32 s29, s17, 0\n"
        "v_mfma_f32_16x16x32_bf16 v[100:103], v[176:179], v[200:203], v[100:103]\n"
        "v_mfma_f32_16x16x32_bf16 v[104:107], v[172:175], v[204:207], v[104:107]\n"
        "global_load_lds_dwordx4 v12, s[28:29]\n"
        "v_mfma_f32_16x16x32_bf16 v[108:111], v[176:179], v[204:207], v[108:111]\n"
        "v_mfma_f32_16x16x32_bf16 v[112:115], v[172:175], v[208:211], v[112:115]\n"
        "global_load_lds_dwordx4 v13, s[28:29] offset:1024\n"
        "v_mfma_f32_16x16x32_bf16 v[116:119], v[176:179], v[208:211], v[116:119]\n"
        "v_mfma_f32_16x16x32_bf16 v[120:123], v[172:175], v[212:215], v[120:123]\n"
        "s_waitcnt vmcnt(13)\n"
        "ds_read_b128 a[0:3], v28 offset:0\n"
        "v_mfma_f32_16x16x32_bf16 v[124:127], v[176:179], v[212:215], v[124:127]\n"
        "s_waitcnt lgkmcnt(1)\n"
        "v_mfma_f32_16x16x32_bf16 v[56:59], v[128:131], v[136:139], v[56:59]\n"
        "ds_read_b128 v[172:175], v25 offset:0\n"
        "ds_read_b128 v[176:179], v25 offset:512\n"
        "v_mfma_f32_16x16x32_bf16 v[60:63], v[132:135], v[136:139], v[60:63]\n"
        "ds_read_b128 v[180:183], v27 offset:0\n"
        "ds_read_b128 v[184:187], v27 offset:2048\n"
        "v_mfma_f32_16x16x32_bf16 v[64:67], v[128:131], v[140:143], v[64:67]\n"
        "ds_read_b128 v[188:191], v27 offset:4096\n"
        "ds_read_b128 v[192:195], v27 offset:6144\n"
        "v_mfma_f32_16x16x32_bf16 v[68:71], v[132:135], v[140:143], v[68:71]\n"
        "ds_read_b128 v[196:199], v27 offset:8192\n"
        "ds_read_b128 v[200:203], v27 offset:10240\n"
        "v_mfma_f32_16x16x32_bf16 v[72:75], v[128:131], v[144:147], v[72:75]\n"
        "ds_read_b128 v[204:207], v27 offset:12288\n"
        "ds_read_b128 v[208:211], v27 offset:14336\n"
        "v_mfma_f32_16x16x32_bf16 v[76:79], v[132:135], v[144:147], v[76:79]\n"
        "ds_read_b128 v[212:215], v27 offset:16384\n"
        "ds_read_b128 a[16:19], v29 offset:0\n"
        "v_mfma_f32_16x16x32_bf16 v[80:83], v[128:131], v[148:151], v[80:83]\n"
        "s_waitcnt vmcnt(10)\n"
        "ds_read_b128 a[32:35], v28 offset:2048\n"
        "ds_read_b128 a[48:51], v29 offset:2048\n"
        "v_mfma_f32_16x16x32_bf16 v[84:87], v[132:135], v[148:151], v[84:87]\n"
        "s_waitcnt lgkmcnt(2)\n"
        "s_add_u32 m0, s60, 0\n"
        "s_add_u32 s28, s16, s15\n"
        "s_addc_u32 s29, s17, 0\n"
        "global_load_lds_dwordx4 v12, s[28:29]\n"
        "global_load_lds_dwordx4 v13, s[28:29] offset:1024\n"
        "v_mfma_f32_16x16x32_bf16 v[56:59], v[172:175], v[180:183], v[56:59]\n"
        "v_mfma_f32_16x16x32_bf16 v[60:63], v[176:179], v[180:183], v[60:63]\n"
        "v_mfma_f32_16x16x32_bf16 v[88:91], v[128:131], v[152:155], v[88:91]\n"
        "v_mfma_f32_16x16x32_bf16 v[92:95], v[132:135], v[152:155], v[92:95]\n"
        "v_mfma_f32_16x16x32_bf16 v[64:67], v[172:175], v[184:187], v[64:67]\n"
        "v_mfma_f32_16x16x32_bf16 v[68:71], v[176:179], v[184:187], v[68:71]\n"
        "v_mfma_f32_16x16x32_bf16 v[96:99], v[128:131], v[156:159], v[96:99]\n"
        "v_mfma_f32_16x16x32_bf16 v[100:103], v[132:135], v[156:159], v[100:103]\n"
        "v_mfma_f32_16x16x32_bf16 v[72:75], v[172:175], v[188:191], v[72:75]\n"
        "v_cvt_pk_bf16_f32 v216, v56, v57\n"
        "v_cvt_pk_bf16_f32 v217, v58, v59\n"
        "v_cvt_pk_bf16_f32 v218, v60, v61\n"
        "v_mfma_f32_16x16x32_bf16 v[76:79], v[176:179], v[188:191], v[76:79]\n"
        "v_cvt_pk_bf16_f32 v219, v62, v63\n"
        "global_store_dwordx4 v44, v[216:219], s[36:37] sc1\n"
        "s_add_u32 s36, s36, 0x10080\n"
        "s_addc_u32 s37, s37, 0\n"
        "v_mfma_f32_16x16x32_bf16 v[104:107], v[128:131], v[160:163], v[104:107]\n"
        "v_mfma_f32_16x16x32_bf16 v[108:111], v[132:135], v[160:163], v[108:111]\n"
        "v_mfma_f32_16x16x32_bf16 v[80:83], v[172:175], v[192:195], v[80:83]\n"
        "v_cvt_pk_bf16_f32 v220, v64, v65\n"
        "v_cvt_pk_bf16_f32 v221, v66, v67\n"
        "v_cvt_pk_bf16_f32 v222, v68, v69\n"
        "v_mfma_f32_16x16x32_bf16 v[84:87], v[176:179], v[192:195], v[84:87]\n"
        "v_cvt_pk_bf16_f32 v223, v70, v71\n"
        "global_store_dwordx4 v44, v[220:223], s[36:37] sc1\n"
        "s_add_u32 s36, s36, 0x10080\n"
        "s_addc_u32 s37, s37, 0\n"
        "v_mfma_f32_16x16x32_bf16 v[112:115], v[128:131], v[164:167], v[112:115]\n"
        "v_mfma_f32_16x16x32_bf16 v[116:119], v[132:135], v[164:167], v[116:119]\n"
        "v_mfma_f32_16x16x32_bf16 v[88:91], v[172:175], v[196:199], v[88:91]\n"
        "v_cvt_pk_bf16_f32 v224, v72, v73\n"
        "v_cvt_pk_bf16_f32 v225, v74, v75\n"
        "v_cvt_pk_bf16_f32 v226, v76, v77\n"
        "v_mfma_f32_16x16x32_bf16 v[92:95], v[176:179], v[196:199], v[92:95]\n"
        "v_cvt_pk_bf16_f32 v227, v78, v79\n"
        "global_store_dwordx4 v44, v[224:227], s[36:37] sc1\n"
        "s_add_u32 s36, s36, 0x10080\n"
        "s_addc_u32 s37, s37, 0\n"
        "v_mfma_f32_16x16x32_bf16 v[120:123], v[128:131], v[168:171], v[120:123]\n"
        "v_mfma_f32_16x16x32_bf16 v[124:127], v[132:135], v[168:171], v[124:127]\n"
        "v_mfma_f32_16x16x32_bf16 v[96:99], v[172:175], v[200:203], v[96:99]\n"
        "v_cvt_pk_bf16_f32 v228, v80, v81\n"
        "v_cvt_pk_bf16_f32 v229, v82, v83\n"
        "v_cvt_pk_bf16_f32 v230, v84, v85\n"
        "v_mfma_f32_16x16x32_bf16 v[100:103], v[176:179], v[200:203], v[100:103]\n"
        "v_cvt_pk_bf16_f32 v231, v86, v87\n"
        "global_store_dwordx4 v44, v[228:231], s[36:37] sc1\n"
        "s_add_u32 s36, s36, 0x10080\n"
        "s_addc_u32 s37, s37, 0\n"
        "v_mfma_f32_16x16x32_bf16 v[104:107], v[172:175], v[204:207], v[104:107]\n"
        "v_cvt_pk_bf16_f32 v232, v88, v89\n"
        "v_cvt_pk_bf16_f32 v233, v90, v91\n"
        "v_cvt_pk_bf16_f32 v234, v92, v93\n"
        "v_mfma_f32_16x16x32_bf16 v[108:111], v[176:179], v[204:207], v[108:111]\n"
        "v_cvt_pk_bf16_f32 v235, v94, v95\n"
        "global_store_dwordx4 v44, v[232:235], s[36:37] sc1\n"
        "s_add_u32 s36, s36, 0x10080\n"
        "s_addc_u32 s37, s37, 0\n"
        "v_mfma_f32_16x16x32_bf16 v[112:115], v[172:175], v[208:211], v[112:115]\n"
        "v_cvt_pk_bf16_f32 v236, v96, v97\n"
        "v_cvt_pk_bf16_f32 v237, v98, v99\n"
        "v_cvt_pk_bf16_f32 v238, v100, v101\n"
        "v_mfma_f32_16x16x32_bf16 v[116:119], v[176:179], v[208:211], v[116:119]\n"
        "v_cvt_pk_bf16_f32 v239, v102, v103\n"
        "global_store_dwordx4 v44, v[236:239], s[36:37] sc1\n"
        "s_add_u32 s36, s36, 0x10080\n"
        "s_addc_u32 s37, s37, 0\n"
        "v_mfma_f32_16x16x32_bf16 v[120:123], v[172:175], v[212:215], v[120:123]\n"
        "v_cvt_pk_bf16_f32 v240, v104, v105\n"
        "v_cvt_pk_bf16_f32 v241, v106, v107\n"
        "v_cvt_pk_bf16_f32 v242, v108, v109\n"
        "v_mfma_f32_16x16x32_bf16 v[124:127], v[176:179], v[212:215], v[124:127]\n"
        "v_cvt_pk_bf16_f32 v243, v110, v111\n"
        "global_store_dwordx4 v44, v[240:243], s[36:37] sc1\n"
        "s_add_u32 s36, s36, 0x10080\n"
        "s_addc_u32 s37, s37, 0\n"
        "s_nop 3\n"
        "v_cvt_pk_bf16_f32 v244, v112, v113\n"
        "v_cvt_pk_bf16_f32 v245, v114, v115\n"
        "v_cvt_pk_bf16_f32 v246, v116, v117\n"
        "v_cvt_pk_bf16_f32 v247, v118, v119\n"
        "global_store_dwordx4 v44, v[244:247], s[36:37] sc1\n"
        "s_add_u32 s36, s36, 0x10080\n"
        "s_addc_u32 s37, s37, 0\n"
        "v_cvt_pk_bf16_f32 v248, v120, v121\n"
        "v_cvt_pk_bf16_f32 v249, v122, v123\n"
        "v_cvt_pk_bf16_f32 v250, v124, v125\n"
        "v_cvt_pk_bf16_f32 v251, v126, v127\n"
        "global_store_dwordx4 v44, v[248:251], s[36:37] sc1\n"
        "s_waitcnt vmcnt(19)\n"
        "ds_read_b128 a[64:67], v28 offset:4096\n"
        "ds_read_b128 a[80:83], v29 offset:4096\n"
        "s_waitcnt lgkmcnt(2)\n"
        "s_add_u32 m0, s60, 2048\n"
        "s_add_u32 s28, s16, s8\n"
        "s_addc_u32 s29, s17, 0\n"
        "global_load_lds_dwordx4 v14, s[28:29]\n"
        "global_load_lds_dwordx4 v15, s[28:29] offset:1024\n"
        "s_waitcnt vmcnt(19)\n"
        "ds_read_b128 a[96:99], v28 offset:6144\n"
        "ds_read_b128 a[112:115], v29 offset:6144\n"
        "s_waitcnt lgkmcnt(2)\n"
        "s_add_u32 m0, s60, 4096\n"
        "s_add_u32 s28, s16, s9\n"
        "s_addc_u32 s29, s17, 0\n"
        "global_load_lds_dwordx4 v14, s[28:29]\n"
        "global_load_lds_dwordx4 v15, s[28:29] offset:1024\n"
        "s_waitcnt vmcnt(19)\n"
        "ds_read_b128 a[128:131], v28 offset:8192\n"
        "ds_read_b128 a[144:147], v29 offset:8192\n"
        "s_waitcnt lgkmcnt(2)\n"
        "s_add_u32 m0, s60, 6144\n"
        "s_add_u32 s28, s16, s10\n"
        "s_addc_u32 s29, s17, 0\n"
        "global_load_lds_dwordx4 v14, s[28:29]\n"
        "global_load_lds_dwordx4 v15, s[28:29] offset:1024\n"
        "s_waitcnt vmcnt(19)\n"
        "ds_read_b128 a[160:163], v28 offset:10240\n"
        "ds_read_b128 a[176:179], v29 offset:10240\n"
        "s_waitcnt lgkmcnt(2)\n"
        "s_add_u32 m0, s60, 8192\n"
        "s_add_u32 s28, s16, s11\n"
        "s_addc_u32 s29, s17, 0\n"
        "global_load_lds_dwordx4 v14, s[28:29]\n"
        "global_load_lds_dwordx4 v15, s[28:29] offset:1024\n"
        "s_waitcnt vmcnt(19)\n"
        "ds_read_b128 a[192:195], v28 offset:12288\n"
        "ds_read_b128 a[208:211], v29 offset:12288\n"
        "s_waitcnt vmcnt(17)\n"
        "ds_read_b128 a[224:227], v28 offset:0\n"
        "ds_read_b128 a[240:243], v29 offset:0\n"
        "s_waitcnt vmcnt(8)\n"
        "s_barrier\n"
        "s_cmp_eq_u32 s43, 0\n"
        "s_cbranch_scc0 Lw_5\n"
        "s_mov_b64 exec, s[22:23]\n"
        "global_store_dword v34, v33, s[30:31] sc1\n"
        "s_mov_b64 exec, -1\n"
        "Lw_5:\n"
        "s_waitcnt lgkmcnt(4)\n"
        "s_add_u32 m0, s60, 10240\n"
        "s_add_u32 s28, s16, s12\n"
        "s_addc_u32 s29, s17, 0\n"
        "global_load_lds_dwordx4 v14, s[28:29]\n"
        "global_load_lds_dwordx4 v15, s[28:29] offset:1024\n"
        "s_waitcnt lgkmcnt(2)\n"
        "s_add_u32 m0, s60, 12288\n"
        "s_add_u32 s28, s16, s13\n"
        "s_addc_u32 s29, s17, 0\n"
        "global_load_lds_dwordx4 v14, s[28:29]\n"
        "global_load_lds_dwordx4 v15, s[28:29] offset:1024\n"
        "s_waitcnt vmcnt(10)\n"
        "ds_read_b128 a[4:7], v28 offset:2048\n"
        "ds_read_b128 a[20:23], v29 offset:2048\n"
        "s_waitcnt lgkmcnt(2)\n"
        "s_add_u32 m0, s60, 0\n"
        "s_add_u32 s28, s16, s14\n"
        "s_addc_u32 s29, s17, 0\n"
        "global_load_lds_dwordx4 v14, s[28:29]\n"
        "global_load_lds_dwordx4 v15, s[28:29] offset:1024\n"
        "s_waitcnt vmcnt(10)\n"
        "ds_read_b128 a[36:39], v28 offset:4096\n"
        "ds_read_b128 a[52:55], v29 offset:4096\n"
        "s_waitcnt lgkmcnt(2)\n"
        "s_add_u32 m0, s60, 2048\n"
        "s_add_u32 s28, s16, s15\n"
        "s_addc_u32 s29, s17, 0\n"
        "global_load_lds_dwordx4 v14, s[28:29]\n"
        "global_load_lds_dwordx4 v15, s[28:29] offset:1024\n"
        "s_waitcnt vmcnt(10)\n"
        "ds_read_b128 a[68:71], v28 offset:6144\n"
        "ds_read_b128 a[84:87], v29 offset:6144\n"
        "s_waitcnt lgkmcnt(2)\n"
        "s_add_u32 m0, s60, 4096\n"
        "s_add_u32 s28, s16, s8\n"
        "s_addc_u32 s29, s17, 0\n"
        "global_load_lds_dwordx4 v16, s[28:29]\n"
        "global_load_lds_dwordx4 v17, s[28:29] offset:1024\n"
        "s_waitcnt vmcnt(10)\n"
        "ds_read_b128 a[100:103], v28 offset:8192\n"
        "ds_read_b128 a[116:119], v29 offset:8192\n"
        "s_waitcnt lgkmcnt(2)\n"
        "s_add_u32 m0, s60, 6144\n"
        "s_add_u32 s28, s16, s9\n"
        "s_addc_u32 s29, s17, 0\n"
        "global_load_lds_dwordx4 v16, s[28:29]\n"
        "global_load_lds_dwordx4 v17, s[28:29] offset:1024\n"
        "s_waitcnt vmcnt(10)\n"
        "ds_read_b128 a[132:135], v28 offset:10240\n"
        "ds_read_b128 a[148:151], v29 offset:10240\n"
        "s_waitcnt lgkmcnt(2)\n"
        "s_add_u32 m0, s60, 8192\n"
        "s_add_u32 s28, s16, s10\n"
        "s_addc_u32 s29, s17, 0\n"
        "global_load_lds_dwordx4 v16, s[28:29]\n"
        "global_load_lds_dwordx4 v17, s[28:29] offset:1024\n"
        "s_waitcnt vmcnt(10)\n"
        "ds_read_b128 a[164:167], v28 offset:12288\n"
        "ds_read_b128 a[180:183], v29 offset:12288\n"
        "s_waitcnt lgkmcnt(2)\n"
        "s_add_u32 m0, s60, 10240\n"
        "s_add_u32 s28, s16, s11\n"
        "s_addc_u32 s29, s17, 0\n"
        "global_load_lds_dwordx4 v16, s[28:29]\n"
        "global_load_lds_dwordx4 v17, s[28:29] offset:1024\n"
        "s_waitcnt vmcnt(10)\n"
        "ds_read_b128 a[196:199], v28 offset:0\n"
        "ds_read_b128 a[212:215], v29 offset:0\n"
        "s_waitcnt lgkmcnt(2)\n"
        "s_add_u32 m0, s60, 12288\n"
        "s_add_u32 s28, s16, s8\n"
        "s_addc_u32 s29, s17, 0\n"
        "global_load_lds_dwordx4 v18, s[28:29]\n"
        "global_load_lds_dwordx4 v19, s[28:29] offset:1024\n"
        "s_waitcnt vmcnt(10)\n"
        "ds_read_b128 a[228:231], v28 offset:2048\n"
        "ds_read_b128 a[244:247], v29 offset:2048\n"
        "s_waitcnt lgkmcnt(2)\n"
        "s_add_u32 m0, s60, 0\n"
        "s_add_u32 s28, s16, s9\n"
        "s_addc_u32 s29, s17, 0\n"
        "global_load_lds_dwordx4 v18, s[28:29]\n"
        "global_load_lds_dwordx4 v19, s[28:29] offset:1024\n"
        "s_waitcnt vmcnt(10)\n"
        "ds_read_b128 a[8:11], v28 offset:4096\n"
        "ds_read_b128 a[24:27], v29 offset:4096\n"
        "s_waitcnt lgkmcnt(2)\n"
        "s_add_u32 m0, s60, 2048\n"
        "s_add_u32 s28, s16, s10\n"
        "s_addc_u32 s29, s17, 0\n"
        "global_load_lds_dwordx4 v18, s[28:29]\n"
        "global_load_lds_dwordx4 v19, s[28:29] offset:1024\n"
        "s_waitcnt vmcnt(10)\n"
        "ds_read_b128 a[40:43], v28 offset:6144\n"
        "ds_read_b128 a[56:59], v29 offset:6144\n"
        "s_waitcnt lgkmcnt(2)\n"
        "s_add_u32 m0, s60, 4096\n"
        "s_add_u32 s28, s16, s11\n"
        "s_addc_u32 s29, s17, 0\n"
        "global_load_lds_dwordx4 v18, s[28:29]\n"
        "global_load_lds_dwordx4 v19, s[28:29] offset:1024\n"
        "s_waitcnt vmcnt(10)\n"
        "ds_read_b128 a[72:75], v28 offset:8192\n"
        "ds_read_b128 a[88:91], v29 offset:8192\n"
        "s_waitcnt lgkmcnt(2)\n"
        "s_add_u32 m0, s60, 6144\n"
        "s_add_u32 s28, s16, s12\n"
        "s_addc_u32 s29, s17, 0\n"
        "global_load_lds_dwordx4 v16, s[28:29]\n"
        "global_load_lds_dwordx4 v17, s[28:29] offset:1024\n"
        "s_waitcnt vmcnt(10)\n"
        "ds_read_b128 a[104:107], v28 offset:10240\n"
        "ds_read_b128 a[120:123], v29 offset:10240\n"
        "s_waitcnt lgkmcnt(2)\n"
        "s_add_u32 m0, s60, 8192\n"
        "s_add_u32 s28, s16, s13\n"
        "s_addc_u32 s29, s17, 0\n"
        "global_load_lds_dwordx4 v16, s[28:29]\n"
        "global_load_lds_dwordx4 v17, s[28:29] offset:1024\n"
        "global_load_dword v31, v30, s[30:31] sc1\n"
        "global_load_dword v36, v32, s[30:31] sc1\n"
        "v_lshl_add_u32 v192, s8, 4, v44\n"
        "v_lshl_add_u32 v193, s9, 4, v44\n"
        "v_lshl_add_u32 v194, s10, 4, v44\n"
        "v_lshl_add_u32 v195, s11, 4, v44\n"
        "v_lshl_add_u32 v196, s12, 4, v44\n"
        "v_lshl_add_u32 v197, s13, 4, v44\n"
        "v_lshl_add_u32 v198, s14, 4, v44\n"
        "v_lshl_add_u32 v199, s15, 4, v44\n"
        "s_waitcnt vmcnt(12)\n"
        "ds_read_b128 a[12:15], v28 offset:12288\n"
        "ds_read_b128 a[28:31], v29 offset:12288\n"
        "s_waitcnt lgkmcnt(2)\n"
        "s_add_u32 m0, s60, 10240\n"
        "s_add_u32 s28, s16, s14\n"
        "s_addc_u32 s29, s17, 0\n"
        "global_load_lds_dwordx4 v16, s[28:29]\n"
        "global_load_lds_dwordx4 v17, s[28:29] offset:1024\n"
        "s_waitcnt vmcnt(12)\n"
        "ds_read_b128 a[44:47], v28 offset:0\n"
        "ds_read_b128 a[60:63], v29 offset:0\n"
        "s_waitcnt lgkmcnt(2)\n"
        "s_add_u32 m0, s60, 12288\n"
        "s_add_u32 s28, s16, s15\n"
        "s_addc_u32 s29, s17, 0\n"
        "global_load_lds_dwordx4 v16, s[28:29]\n"
        "global_load_lds_dwordx4 v17, s[28:29] offset:1024\n"
        "s_waitcnt vmcnt(12)\n"
        "ds_read_b128 a[76:79], v28 offset:2048\n"
        "ds_read_b128 a[92:95], v29 offset:2048\n"
        "s_waitcnt lgkmcnt(2)\n"
        "s_add_u32 m0, s60, 0\n"
        "s_add_u32 s28, s16, s12\n"
        "s_addc_u32 s29, s17, 0\n"
        "global_load_lds_dwordx4 v18, s[28:29]\n"
        "global_load_lds_dwordx4 v19, s[28:29] offset:1024\n"
        "s_waitcnt vmcnt(12)\n"
        "ds_read_b128 a[108:111], v28 offset:4096\n"
        "ds_read_b128 a[124:127], v29 offset:4096\n"
        "s_waitcnt lgkmcnt(2)\n"
        "s_add_u32 m0, s60, 2048\n"
        "s_add_u32 s28, s16, s13\n"
        "s_addc_u32 s29, s17, 0\n"
        "global_load_lds_dwordx4 v18, s[28:29]\n"
        "global_load_lds_dwordx4 v19, s[28:29] offset:1024\n"
        "s_waitcnt vmcnt(12)\n"
        "ds_read_b128 a[136:139], v28 offset:6144\n"
        "ds_read_b128 a[152:155], v29 offset:6144\n"
        "s_waitcnt lgkmcnt(2)\n"
        "s_add_u32 m0, s60, 4096\n"
        "s_add_u32 s28, s16, s14\n"
        "s_addc_u32 s29, s17, 0\n"
        "global_load_lds_dwordx4 v18, s[28:29]\n"
        "global_load_lds_dwordx4 v19, s[28:29] offset:1024\n"
        "s_waitcnt vmcnt(12)\n"
        "ds_read_b128 a[168:171], v28 offset:8192\n"
        "ds_read_b128 a[184:187], v29 offset:8192\n"
        "s_waitcnt lgkmcnt(2)\n"
        "s_add_u32 m0, s60, 6144\n"
        "s_add_u32 s28, s16, s15\n"
        "s_addc_u32 s29, s17, 0\n"
        "global_load_lds_dwordx4 v18, s[28:29]\n"
        "global_load_lds_dwordx4 v19, s[28:29] offset:1024\n"
        "s_waitcnt vmcnt(12)\n"
        "s_waitcnt lgkmcnt(0)\n"
        "s_branch Lmerge\n"
        "Lpref:\n"
        "v_lshl_add_u32 v54, v2, 12, v53\n"
        "s_add_u32 s51, s43, 0\n"
        "s_and_b32 s51, s51, 3\n"
        "s_lshl_b32 s51, s51, 16\n"
        "v_add_u32 v12, s51, v54\n"
        "v_add_u32 v13, 31744, v12\n"
        "s_add_u32 s51, s43, 1\n"
        "s_and_b32 s51, s51, 3\n"
        "s_lshl_b32 s51, s51, 16\n"
        "v_add_u32 v14, s51, v54\n"
        "v_add_u32 v15, 31744, v14\n"
        "s_add_u32 s51, s43, 2\n"
        "s_and_b32 s51, s51, 3\n"
        "s_lshl_b32 s51, s51, 16\n"
        "v_add_u32 v16, s51, v54\n"
        "v_add_u32 v17, 31744, v16\n"
        "s_add_u32 s51, s43, 3\n"
        "s_and_b32 s51, s51, 3\n"
        "s_lshl_b32 s51, s51, 16\n"
        "v_add_u32 v18, s51, v54\n"
        "v_add_u32 v19, 31744, v18\n"
        "s_lshl2_add_u32 s51, s45, s50\n"
        "s_lshl_b32 s51, s51, 7\n"
        "s_add_u32 s8, s51, 0\n"
        "s_and_b32 s8, s8, 0x380\n"
        "s_add_u32 s9, s51, 128\n"
        "s_and_b32 s9, s9, 0x380\n"
        "s_add_u32 s10, s51, 256\n"
        "s_and_b32 s10, s10, 0x380\n"
        "s_add_u32 s11, s51, 384\n"
        "s_and_b32 s11, s11, 0x380\n"
        "s_xor_b32 s12, s8, 512\n"
        "s_xor_b32 s13, s9, 512\n"
        "s_xor_b32 s14, s10, 512\n"
        "s_xor_b32 s15, s11, 512\n"
        "s_lshl_b32 s57, s46, 18\n"
        "s_lshl_b32 s52, s43, 10\n"
        "s_add_u32 s57, s57, s52\n"
        "s_lshl_b32 s18, s47, 5\n"
        "s_add_u32 s18, s18, s46\n"
        "s_lshr_b32 s51, s18, 3\n"
        "s_lshl_b32 s51, s51, 7\n"
        "s_lshl2_add_u32 s51, s50, s51\n"
        "s_add_u32 s52, s51, 0x7b0000\n"
        "v_mov_b32 v35, s52\n"
        "s_lshl_b32 s19, s18, 10\n"
        "s_add_u32 s19, s19, 0x7c0000\n"
        "s_add_u32 s62, s19, 512\n"
        "v_mov_b32 v30, s62\n"
        "s_add_u32 s62, s19, 768\n"
        "v_writelane_b32 v30, s62, 14\n"
        "s_add_u32 s62, s19, 640\n"
        "v_writelane_b32 v30, s62, 13\n"
        "s_and_b32 s51, s45, 1\n"
        "s_lshl2_add_u32 s51, s44, s51\n"
        "s_lshl2_add_u32 s51, s51, s43\n"
        "s_lshr_b32 s62, s45, 1\n"
        "s_lshl3_add_u32 s51, s51, s62\n"
        "s_lshl_b32 s51, s51, 10\n"
        "s_lshl2_add_u32 s51, s46, s51\n"
        "s_add_u32 s51, s51, 0x7c0000\n"
        "v_lshl_add_u32 v54, v1, 11, s51\n"
        "v_cmp_gt_u32 vcc, 4, v1\n"
        "s_nop 1\n"
        "v_cndmask_b32 v30, v30, v54, vcc\n"
        "v_mov_b32 v36, v30\n"
        "v_writelane_b32 v36, s52, 17\n"
        "s_cmp_eq_u32 s44, 0\n"
        "s_cselect_b64 s[28:29], s[66:67], s[68:69]\n"
        "s_add_u32 s16, s28, s57\n"
        "s_addc_u32 s17, s29, 0\n"
        "v_lshlrev_b32 v54, 16, v1\n"
        "s_mov_b64 exec, 15\n"
        "global_load_dword v239, v54, s[16:17]\n"
        "s_mov_b64 exec, -1\n"
        "s_mov_b64 s[30:31], s[76:77]\n"
        "global_load_dword v31, v36, s[30:31] sc1\n"
        "s_mul_i32 s51, s46, 73728\n"
        "s_add_u32 s54, s74, s51\n"
        "s_addc_u32 s55, s75, 0\n"
        "v_and_b32 v54, 1, v1\n"
        "v_lshlrev_b32 v54, 16, v54\n"
        "s_mov_b64 exec, 3\n"
        "global_load_dword v251, v54, s[54:55] sc1\n"
        "s_mov_b64 exec, -1\n"
        "v_mul_u32_u24 v54, 0x10080, v1\n"
        "s_mul_i32 s51, s47, 590976\n"
        "s_add_u32 s51, s51, 0x10000\n"
        "v_add_u32 v55, s51, v54\n"
        "s_mul_i32 s51, s61, 3200\n"
        "s_add_u32 s51, s51, 0x490c00\n"
        "v_writelane_b32 v55, s51, 9\n"
        "s_xor_b32 s51, s61, 16\n"
        "s_mul_i32 s51, s51, 3200\n"
        "s_add_u32 s51, s51, 0x490c00\n"
        "v_writelane_b32 v55, s51, 10\n"
        "s_mov_b64 exec, 0x7ff\n"
        "global_load_dword v250, v55, s[30:31] sc1\n"
        "s_mov_b64 exec, -1\n"
        "s_add_u32 m0, s60, 0\n"
        "s_add_u32 s28, s16, s8\n"
        "s_addc_u32 s29, s17, 0\n"
        "global_load_lds_dwordx4 v12, s[28:29]\n"
        "global_load_lds_dwordx4 v13, s[28:29] offset:1024\n"
        "s_add_u32 m0, s60, 2048\n"
        "s_add_u32 s28, s16, s9\n"
        "s_addc_u32 s29, s17, 0\n"
        "global_load_lds_dwordx4 v12, s[28:29]\n"
        "global_load_lds_dwordx4 v13, s[28:29] offset:1024\n"
        "s_add_u32 m0, s60, 4096\n"
        "s_add_u32 s28, s16, s10\n"
        "s_addc_u32 s29, s17, 0\n"
        "global_load_lds_dwordx4 v12, s[28:29]\n"
        "global_load_lds_dwordx4 v13, s[28:29] offset:1024\n"
        "s_add_u32 m0, s60, 6144\n"
        "s_add_u32 s28, s16, s11\n"
        "s_addc_u32 s29, s17, 0\n"
        "global_load_lds_dwordx4 v12, s[28:29]\n"
        "global_load_lds_dwordx4 v13, s[28:29] offset:1024\n"
        "v_lshrrev_b32 v2, 4, v1\n"
        "v_and_b32 v53, 15, v1\n"
        "v_xor_b32 v54, v2, v3\n"
        "v_lshlrev_b32 v54, 4, v54\n"
        "v_lshl_add_u32 v54, v53, 7, v54\n"
        "v_add_u32 v28, s60, v54\n"
        "v_xor_b32 v29, 64, v28\n"
        "v_lshlrev_b32 v44, 4, v1\n"
        "s_mul_i32 s51, s61, 3200\n"
        "s_add_u32 s51, s51, 0x490000\n"
        "s_add_u32 s38, s30, s51\n"
        "s_addc_u32 s39, s31, 0\n"
        "v_add_u32 v47, s60, v44\n"
        "s_sub_u32 s51, s43, 1\n"
        "s_and_b32 s51, s51, 3\n"
        "s_mul_i32 s51, s51, 14336\n"
        "s_add_u32 s51, s51, 104448\n"
        "v_add_u32 v48, s51, v44\n"
        "s_sub_u32 s51, s43, 2\n"
        "s_and_b32 s51, s51, 3\n"
        "s_mul_i32 s51, s51, 14336\n"
        "s_add_u32 s51, s51, 105472\n"
        "v_add_u32 v49, s51, v44\n"
        "s_sub_u32 s51, s43, 3\n"
        "s_and_b32 s51, s51, 3\n"
        "s_mul_i32 s51, s51, 14336\n"
        "s_add_u32 s51, s51, 106496\n"
        "v_add_u32 v50, s51, v44\n"
        "s_mul_i32 s51, s43, 18432\n"
        "v_mul_u32_u24 v52, 0x480, v53\n"
        "v_lshl_add_u32 v52, v2, 4, v52\n"
        "v_add_u32 v52, s51, v52\n"
        "s_mul_i32 s51, s46, 73728\n"
        "s_mul_i32 s52, s45, 288\n"
        "s_add_u32 s51, s51, s52\n"
        "s_add_u32 s34, s74, s51\n"
        "s_addc_u32 s35, s75, 0\n"
        "s_mul_i32 s51, s47, 590976\n"
        "s_lshl_b32 s52, s43, 14\n"
        "s_add_u32 s52, s52, s51\n"
        "s_cmp_eq_u32 s44, 0\n"
        "s_cselect_b32 s62, 328320, 0\n"
        "s_add_u32 s52, s52, s62\n"
        "s_add_u32 s78, s30, s52\n"
        "s_addc_u32 s79, s31, 0\n"
        "s_xor_b32 s51, s61, 16\n"
        "s_mul_i32 s51, s51, 3200\n"
        "s_add_u32 s51, s51, 0x490000\n"
        "s_add_u32 s40, s30, s51\n"
        "s_addc_u32 s41, s31, 0\n"
        "s_and_b32 s51, s49, 3\n"
        "s_lshl2_add_u32 s51, s44, s51\n"
        "s_lshl_b32 s51, s51, 9\n"
        "s_add_u32 s51, s51, 0x7b0000\n"
        "v_bfe_u32 v54, v1, 3, 2\n"
        "v_lshlrev_b32 v54, 7, v54\n"
        "v_lshl_add_u32 v54, v3, 2, v54\n"
        "v_add_u32 v32, s51, v54\n"
        "s_and_b32 s51, s45, 1\n"
        "s_lshl2_add_u32 s51, s44, s51\n"
        "s_lshl_b32 s51, s51, 9\n"
        "s_lshr_b32 s52, s45, 1\n"
        "s_lshl2_add_u32 s51, s52, s51\n"
        "s_add_u32 s51, s51, 0x7b0000\n"
        "v_bfe_u32 v54, v1, 2, 2\n"
        "v_lshlrev_b32 v54, 7, v54\n"
        "v_and_b32 v55, 3, v1\n"
        "v_lshl_add_u32 v54, v55, 3, v54\n"
        "v_add_u32 v54, s51, v54\n"
        "v_cmp_gt_u32 vcc, 32, v1\n"
        "s_nop 1\n"
        "v_cndmask_b32 v32, v54, v32, vcc\n"
        "v_xor_b32 v54, 0x800, v35\n"
        "v_cmp_gt_u32 vcc, 48, v1\n"
        "s_nop 1\n"
        "v_cndmask_b32 v32, v54, v32, vcc\n"
        "v_lshl_add_u32 v34, v1, 2, s19\n"
        "s_xor_b32 s51, s18, 128\n"
        "s_lshl_b32 s51, s51, 10\n"
        "s_add_u32 s51, s51, 0x7c0200\n"
        "v_writelane_b32 v34, s51, 48\n"
        "s_mov_b32 s22, 0\n"
        "s_mov_b32 s23, 0x7ffff\n"
        "v_readfirstlane_b32 s52, v35\n"
        "s_xor_b32 s51, s18, 64\n"
        "s_lshl_b32 s51, s51, 10\n"
        "s_add_u32 s51, s51, 0x7c0300\n"
        "v_writelane_b32 v34, s51, 50\n"
        "s_xor_b32 s52, s52, 0x400\n"
        "v_writelane_b32 v32, s52, 50\n"
        "v_readfirstlane_b32 s52, v35\n"
        "s_xor_b32 s51, s18, 192\n"
        "s_lshl_b32 s51, s51, 10\n"
        "s_add_u32 s51, s51, 0x7c0200\n"
        "v_writelane_b32 v34, s51, 49\n"
        "s_xor_b32 s52, s52, 0xc00\n"
        "v_writelane_b32 v32, s52, 49\n"
        "s_waitcnt vmcnt(10)\n"
        "v_readlane_b32 s48, v31, 17\n"
        "s_lshl3_add_u32 s51, s43, 161792\n"
        "v_mov_b32 v39, s51\n"
        "v_mov_b32 v36, 161792\n"
        "s_add_u32 s48, s48, 1\n"
        "v_mov_b32 v54, s48\n"
        "s_cmp_eq_u32 s43, 0\n"
        "s_cbranch_scc0 Lw_6\n"
        "s_mov_b64 exec, 1\n"
        "global_store_dword v35, v54, s[30:31] sc1\n"
        "s_mov_b64 exec, -1\n"
        "Lw_6:\n"
        "v_cmp_eq_u32 vcc, s48, v31\n"
        "v_mov_b32 v42, s48\n"
        "s_nop 3\n"
        "s_cmp_lg_u64 vcc, 0\n"
        "s_cselect_b32 s51, 1, 0\n"
        "v_mov_b32 v43, s51\n"
        "ds_write_b64 v39, v[42:43]\n"
        "s_waitcnt lgkmcnt(0)\n"
        "s_barrier\n"
        "ds_read_b128 v[224:227], v36\n"
        "ds_read_b128 v[228:231], v36 offset:16\n"
        "s_waitcnt lgkmcnt(0)\n"
        "Ln1_7:\n"
        "v_xor_b32 v226, v224, v226\n"
        "v_xor_b32 v228, v224, v228\n"
        "v_xor_b32 v230, v224, v230\n"
        "v_or3_b32 v42, v225, v226, v227\n"
        "v_or3_b32 v42, v42, v228, v229\n"
        "v_or3_b32 v42, v42, v230, v231\n"
        "v_cmp_ne_u32 vcc, 0, v42\n"
        "s_nop 4\n"
        "s_cbranch_vccz Ln1ok_8\n"
        "v_readfirstlane_b32 s48, v224\n"
        "s_barrier\n"
        "s_nop 1\n"
        "s_add_u32 s48, s48, 1\n"
        "v_mov_b32 v54, s48\n"
        "s_cmp_eq_u32 s43, 0\n"
        "s_cbranch_scc0 Lw_9\n"
        "s_mov_b64 exec, 1\n"
        "global_store_dword v35, v54, s[30:31] sc1\n"
        "s_mov_b64 exec, -1\n"
        "Lw_9:\n"
        "v_cmp_eq_u32 vcc, s48, v31\n"
        "v_mov_b32 v42, s48\n"
        "s_nop 3\n"
        "s_cmp_lg_u64 vcc, 0\n"
        "s_cselect_b32 s51, 1, 0\n"
        "v_mov_b32 v43, s51\n"
        "ds_write_b64 v39, v[42:43]\n"
        "s_waitcnt lgkmcnt(0)\n"
        "s_barrier\n"
        "ds_read_b128 v[224:227], v36\n"
        "ds_read_b128 v[228:231], v36 offset:16\n"
        "s_waitcnt lgkmcnt(0)\n"
        "s_branch Ln1_7\n"
        "Ln1ok_8:\n"
        "v_mov_b32 v216, -1\n"
        "v_mov_b32 v217, -1\n"
        "v_mov_b32 v218, -1\n"
        "v_mov_b32 v219, -1\n"
        "s_sub_u32 s54, s38, 204800\n"
        "s_subb_u32 s55, s39, 0\n"
        "s_cmp_eq_u32 s44, 0\n"
        "s_cbranch_scc0 Lc_10\n"
        "global_store_dwordx4 v44, v[216:219], s[38:39] offset:2048 sc1\n"
        "global_store_dwordx4 v44, v[216:219], s[54:55] offset:2048 sc1\n"
        "Lc_10:\n"
        "global_store_dwordx4 v44, v[216:219], s[38:39] offset:0 sc1\n"
        "global_store_dwordx4 v44, v[216:219], s[38:39] offset:1024 sc1\n"
        "global_store_dwordx4 v44, v[216:219], s[54:55] offset:0 sc1\n"
        "global_store_dwordx4 v44, v[216:219], s[54:55] offset:1024 sc1\n"
        "s_add_u32 m0, s60, 8192\n"
        "s_add_u32 s28, s16, s8\n"
        "s_addc_u32 s29, s17, 0\n"
        "global_load_lds_dwordx4 v14, s[28:29]\n"
        "global_load_lds_dwordx4 v15, s[28:29] offset:1024\n"
        "s_add_u32 m0, s60, 10240\n"
        "s_add_u32 s28, s16, s9\n"
        "s_addc_u32 s29, s17, 0\n"
        "global_load_lds_dwordx4 v14, s[28:29]\n"
        "global_load_lds_dwordx4 v15, s[28:29] offset:1024\n"
        "s_add_u32 m0, s60, 12288\n"
        "s_add_u32 s28, s16, s10\n"
        "s_addc_u32 s29, s17, 0\n"
        "global_load_lds_dwordx4 v14, s[28:29]\n"
        "global_load_lds_dwordx4 v15, s[28:29] offset:1024\n"
        "s_waitcnt vmcnt(16)\n"
        "ds_read_b128 a[0:3], v28 offset:0\n"
        "ds_read_b128 a[16:19], v29 offset:0\n"
        "s_waitcnt vmcnt(14)\n"
        "ds_read_b128 a[32:35], v28 offset:2048\n"
        "ds_read_b128 a[48:51], v29 offset:2048\n"
        "s_waitcnt lgkmcnt(2)\n"
        "s_add_u32 m0, s60, 0\n"
        "s_add_u32 s28, s16, s11\n"
        "s_addc_u32 s29, s17, 0\n"
        "global_load_lds_dwordx4 v14, s[28:29]\n"
        "global_load_lds_dwordx4 v15, s[28:29] offset:1024\n"
        "s_waitcnt vmcnt(14)\n"
        "ds_read_b128 a[64:67], v28 offset:4096\n"
        "ds_read_b128 a[80:83], v29 offset:4096\n"
        "s_waitcnt lgkmcnt(2)\n"
        "s_add_u32 m0, s60, 2048\n"
        "s_add_u32 s28, s16, s8\n"
        "s_addc_u32 s29, s17, 0\n"
        "global_load_lds_dwordx4 v16, s[28:29]\n"
        "global_load_lds_dwordx4 v17, s[28:29] offset:1024\n"
        "s_waitcnt vmcnt(8)\n"
        "s_barrier\n"
        "global_load_dword v33, v32, s[30:31] sc1\n"
        "ds_read_b128 a[96:99], v28 offset:6144\n"
        "ds_read_b128 a[112:115], v29 offset:6144\n"
        "s_waitcnt lgkmcnt(2)\n"
        "s_add_u32 m0, s60, 4096\n"
        "s_add_u32 s28, s16, s9\n"
        "s_addc_u32 s29, s17, 0\n"
        "global_load_lds_dwordx4 v16, s[28:29]\n"
        "global_load_lds_dwordx4 v17, s[28:29] offset:1024\n"
        "ds_read_b128 a[4:7], v28 offset:8192\n"
        "ds_read_b128 a[20:23], v29 offset:8192\n"
        "s_waitcnt lgkmcnt(2)\n"
        "s_add_u32 m0, s60, 6144\n"
        "s_add_u32 s28, s16, s10\n"
        "s_addc_u32 s29, s17, 0\n"
        "global_load_lds_dwordx4 v16, s[28:29]\n"
        "global_load_lds_dwordx4 v17, s[28:29] offset:1024\n"
        "s_waitcnt vmcnt(11)\n"
        "ds_read_b128 a[36:39], v28 offset:10240\n"
        "ds_read_b128 a[52:55], v29 offset:10240\n"
        "s_waitcnt lgkmcnt(2)\n"
        "s_add_u32 m0, s60, 8192\n"
        "s_add_u32 s28, s16, s11\n"
        "s_addc_u32 s29, s17, 0\n"
        "global_load_lds_dwordx4 v16, s[28:29]\n"
        "global_load_lds_dwordx4 v17, s[28:29] offset:1024\n"
        "s_waitcnt vmcnt(11)\n"
        "ds_read_b128 a[68:71], v28 offset:12288\n"
        "ds_read_b128 a[84:87], v29 offset:12288\n"
        "s_waitcnt lgkmcnt(2)\n"
        "s_add_u32 m0, s60, 10240\n"
        "s_add_u32 s28, s16, s8\n"
        "s_addc_u32 s29, s17, 0\n"
        "global_load_lds_dwordx4 v18, s[28:29]\n"
        "global_load_lds_dwordx4 v19, s[28:29] offset:1024\n"
        "s_waitcnt vmcnt(11)\n"
        "ds_read_b128 a[100:103], v28 offset:0\n"
        "ds_read_b128 a[116:119], v29 offset:0\n"
        "s_waitcnt lgkmcnt(2)\n"
        "s_add_u32 m0, s60, 12288\n"
        "s_add_u32 s28, s16, s9\n"
        "s_addc_u32 s29, s17, 0\n"
        "global_load_lds_dwordx4 v18, s[28:29]\n"
        "global_load_lds_dwordx4 v19, s[28:29] offset:1024\n"
        "s_waitcnt vmcnt(10)\n"
        "s_cmp_eq_u32 s43, 0\n"
        "s_cbranch_scc0 Lw_11\n"
        "s_mov_b64 exec, s[22:23]\n"
        "global_store_dword v34, v33, s[30:31] sc1\n"
        "s_mov_b64 exec, -1\n"
        "Lw_11:\n"
        "ds_read_b128 a[8:11], v28 offset:2048\n"
        "ds_read_b128 a[24:27], v29 offset:2048\n"
        "s_waitcnt lgkmcnt(2)\n"
        "s_add_u32 m0, s60, 0\n"
        "s_add_u32 s28, s16, s10\n"
        "s_addc_u32 s29, s17, 0\n"
        "global_load_lds_dwordx4 v18, s[28:29]\n"
        "global_load_lds_dwordx4 v19, s[28:29] offset:1024\n"
        "s_waitcnt vmcnt(10)\n"
        "ds_read_b128 a[40:43], v28 offset:4096\n"
        "ds_read_b128 a[56:59], v29 offset:4096\n"
        "s_waitcnt lgkmcnt(2)\n"
        "s_add_u32 m0, s60, 2048\n"
        "s_add_u32 s28, s16, s11\n"
        "s_addc_u32 s29, s17, 0\n"
        "global_load_lds_dwordx4 v18, s[28:29]\n"
        "global_load_lds_dwordx4 v19, s[28:29] offset:1024\n"
        "s_waitcnt vmcnt(10)\n"
        "ds_read_b128 a[72:75], v28 offset:6144\n"
        "ds_read_b128 a[88:91], v29 offset:6144\n"
        "s_waitcnt lgkmcnt(2)\n"
        "s_add_u32 m0, s60, 4096\n"
        "s_add_u32 s28, s16, s12\n"
        "s_addc_u32 s29, s17, 0\n"
        "global_load_lds_dwordx4 v12, s[28:29]\n"
        "global_load_lds_dwordx4 v13, s[28:29] offset:1024\n"
        "s_waitcnt vmcnt(10)\n"
        "ds_read_b128 a[104:107], v28 offset:8192\n"
        "ds_read_b128 a[120:123], v29 offset:8192\n"
        "s_waitcnt lgkmcnt(2)\n"
        "s_add_u32 m0, s60, 6144\n"
        "s_add_u32 s28, s16, s13\n"
        "s_addc_u32 s29, s17, 0\n"
        "global_load_lds_dwordx4 v12, s[28:29]\n"
        "global_load_lds_dwordx4 v13, s[28:29] offset:1024\n"
        "s_waitcnt vmcnt(10)\n"
        "ds_read_b128 a[12:15], v28 offset:10240\n"
        "ds_read_b128 a[28:31], v29 offset:10240\n"
        "s_waitcnt lgkmcnt(2)\n"
        "s_add_u32 m0, s60, 8192\n"
        "s_add_u32 s28, s16, s14\n"
        "s_addc_u32 s29, s17, 0\n"
        "global_load_lds_dwordx4 v12, s[28:29]\n"
        "global_load_lds_dwordx4 v13, s[28:29] offset:1024\n"
        "s_waitcnt vmcnt(10)\n"
        "ds_read_b128 a[44:47], v28 offset:12288\n"
        "ds_read_b128 a[60:63], v29 offset:12288\n"
        "s_waitcnt lgkmcnt(2)\n"
        "s_add_u32 m0, s60, 10240\n"
        "s_add_u32 s28, s16, s15\n"
        "s_addc_u32 s29, s17, 0\n"
        "global_load_lds_dwordx4 v12, s[28:29]\n"
        "global_load_lds_dwordx4 v13, s[28:29] offset:1024\n"
        "s_waitcnt vmcnt(10)\n"
        "ds_read_b128 a[76:79], v28 offset:0\n"
        "ds_read_b128 a[92:95], v29 offset:0\n"
        "s_waitcnt lgkmcnt(2)\n"
        "s_add_u32 m0, s60, 12288\n"
        "s_add_u32 s28, s16, s12\n"
        "s_addc_u32 s29, s17, 0\n"
        "global_load_lds_dwordx4 v14, s[28:29]\n"
        "global_load_lds_dwordx4 v15, s[28:29] offset:1024\n"
        "s_waitcnt vmcnt(10)\n"
        "ds_read_b128 a[108:111], v28 offset:2048\n"
        "ds_read_b128 a[124:127], v29 offset:2048\n"
        "s_waitcnt lgkmcnt(2)\n"
        "s_add_u32 m0, s60, 0\n"
        "s_add_u32 s28, s16, s13\n"
        "s_addc_u32 s29, s17, 0\n"
        "global_load_lds_dwordx4 v14, s[28:29]\n"
        "global_load_lds_dwordx4 v15, s[28:29] offset:1024\n"
        "s_waitcnt vmcnt(10)\n"
        "ds_read_b128 a[128:131], v28 offset:4096\n"
        "ds_read_b128 a[144:147], v29 offset:4096\n"
        "s_waitcnt lgkmcnt(2)\n"
        "s_add_u32 m0, s60, 2048\n"
        "s_add_u32 s28, s16, s14\n"
        "s_addc_u32 s29, s17, 0\n"
        "global_load_lds_dwordx4 v14, s[28:29]\n"
        "global_load_lds_dwordx4 v15, s[28:29] offset:1024\n"
        "s_waitcnt vmcnt(10)\n"
        "ds_read_b128 a[160:163], v28 offset:6144\n"
        "ds_read_b128 a[176:179], v29 offset:6144\n"
        "s_waitcnt lgkmcnt(2)\n"
        "s_add_u32 m0, s60, 4096\n"
        "s_add_u32 s28, s16, s15\n"
        "s_addc_u32 s29, s17, 0\n"
        "global_load_lds_dwordx4 v14, s[28:29]\n"
        "global_load_lds_dwordx4 v15, s[28:29] offset:1024\n"
        "s_waitcnt vmcnt(10)\n"
        "ds_read_b128 a[192:195], v28 offset:8192\n"
        "ds_read_b128 a[208:211], v29 offset:8192\n"
        "s_waitcnt lgkmcnt(2)\n"
        "s_add_u32 m0, s60, 6144\n"
        "s_add_u32 s28, s16, s12\n"
        "s_addc_u32 s29, s17, 0\n"
        "global_load_lds_dwordx4 v16, s[28:29]\n"
        "global_load_lds_dwordx4 v17, s[28:29] offset:1024\n"
        "s_waitcnt vmcnt(10)\n"
        "ds_read_b128 a[224:227], v28 offset:10240\n"
        "ds_read_b128 a[240:243], v29 offset:10240\n"
        "s_waitcnt lgkmcnt(2)\n"
        "s_add_u32 m0, s60, 8192\n"
        "s_add_u32 s28, s16, s13\n"
        "s_addc_u32 s29, s17, 0\n"
        "global_load_lds_dwordx4 v16, s[28:29]\n"
        "global_load_lds_dwordx4 v17, s[28:29] offset:1024\n"
        "global_load_dword v31, v30, s[30:31] sc1\n"
        "global_load_dword v36, v32, s[30:31] sc1\n"
        "v_lshl_add_u32 v192, s8, 4, v44\n"
        "v_lshl_add_u32 v193, s9, 4, v44\n"
        "v_lshl_add_u32 v194, s10, 4, v44\n"
        "v_lshl_add_u32 v195, s11, 4, v44\n"
        "v_lshl_add_u32 v196, s12, 4, v44\n"
        "v_lshl_add_u32 v197, s13, 4, v44\n"
        "v_lshl_add_u32 v198, s14, 4, v44\n"
        "v_lshl_add_u32 v199, s15, 4, v44\n"
        "s_waitcnt vmcnt(12)\n"
        "ds_read_b128 a[132:135], v28 offset:12288\n"
        "ds_read_b128 a[148:151], v29 offset:12288\n"
        "s_waitcnt lgkmcnt(2)\n"
        "s_add_u32 m0, s60, 10240\n"
        "s_add_u32 s28, s16, s14\n"
        "s_addc_u32 s29, s17, 0\n"
        "global_load_lds_dwordx4 v16, s[28:29]\n"
        "global_load_lds_dwordx4 v17, s[28:29] offset:1024\n"
        "s_waitcnt vmcnt(12)\n"
        "ds_read_b128 a[164:167], v28 offset:0\n"
        "ds_read_b128 a[180:183], v29 offset:0\n"
        "s_waitcnt lgkmcnt(2)\n"
        "s_add_u32 m0, s60, 12288\n"
        "s_add_u32 s28, s16, s15\n"
        "s_addc_u32 s29, s17, 0\n"
        "global_load_lds_dwordx4 v16, s[28:29]\n"
        "global_load_lds_dwordx4 v17, s[28:29] offset:1024\n"
        "s_waitcnt vmcnt(12)\n"
        "ds_read_b128 a[196:199], v28 offset:2048\n"
        "ds_read_b128 a[212:215], v29 offset:2048\n"
        "s_waitcnt lgkmcnt(2)\n"
        "s_add_u32 m0, s60, 0\n"
        "s_add_u32 s28, s16, s12\n"
        "s_addc_u32 s29, s17, 0\n"
        "global_load_lds_dwordx4 v18, s[28:29]\n"
        "global_load_lds_dwordx4 v19, s[28:29] offset:1024\n"
        "s_waitcnt vmcnt(12)\n"
        "ds_read_b128 a[228:231], v28 offset:4096\n"
        "ds_read_b128 a[244:247], v29 offset:4096\n"
        "s_waitcnt lgkmcnt(2)\n"
        "s_add_u32 m0, s60, 2048\n"
        "s_add_u32 s28, s16, s13\n"
        "s_addc_u32 s29, s17, 0\n"
        "global_load_lds_dwordx4 v18, s[28:29]\n"
        "global_load_lds_dwordx4 v19, s[28:29] offset:1024\n"
        "s_waitcnt vmcnt(12)\n"
        "ds_read_b128 a[136:139], v28 offset:6144\n"
        "ds_read_b128 a[152:155], v29 offset:6144\n"
        "s_waitcnt lgkmcnt(2)\n"
        "s_add_u32 m0, s60, 4096\n"
        "s_add_u32 s28, s16, s14\n"
        "s_addc_u32 s29, s17, 0\n"
        "global_load_lds_dwordx4 v18, s[28:29]\n"
        "global_load_lds_dwordx4 v19, s[28:29] offset:1024\n"
        "s_waitcnt vmcnt(12)\n"
        "ds_read_b128 a[168:171], v28 offset:8192\n"
        "ds_read_b128 a[184:187], v29 offset:8192\n"
        "s_waitcnt lgkmcnt(2)\n"
        "s_add_u32 m0, s60, 6144\n"
        "s_add_u32 s28, s16, s15\n"
        "s_addc_u32 s29, s17, 0\n"
        "global_load_lds_dwordx4 v18, s[28:29]\n"
        "global_load_lds_dwordx4 v19, s[28:29] offset:1024\n"
        "s_waitcnt vmcnt(12)\n"
        "s_waitcnt lgkmcnt(0)\n"
        "Lmerge:\n"
        "s_mov_b32 s53, 0x40000\n"
        "Lpoll_12:\n"
        "v_cmp_ne_u32 vcc, v36, v33\n"
        "s_nop 4\n"
        "s_cbranch_vccz Lkeep_14\n"
        "v_mov_b32 v33, v36\n"
        "s_cmp_eq_u32 s43, 0\n"
        "s_cbranch_scc0 Lw_15\n"
        "s_mov_b64 exec, s[22:23]\n"
        "global_store_dword v34, v33, s[30:31] sc1\n"
        "s_mov_b64 exec, -1\n"
        "Lw_15:\n"
        "Lkeep_14:\n"
        "v_cmp_ne_u32 vcc, s48, v31\n"
        "s_nop 4\n"
        "s_cbranch_vccz Lpollok_13\n"
        "s_sleep 2\n"
        "global_load_dword v31, v30, s[30:31] sc1\n"
        "global_load_dword v36, v32, s[30:31] sc1\n"
        "s_waitcnt vmcnt(0)\n"
        "s_sub_u32 s53, s53, 1\n"
        "s_cbranch_scc0 Lpoll_12\n"
        "Lpollok_13:\n"
        "s_cmp_eq_u32 s44, 1\n"
        "s_cbranch_scc1 Lh1\n"
        "global_load_dwordx4 v[96:99], v192, s[78:79] offset:0 sc1\n"
        "global_load_dwordx4 v[100:103], v192, s[78:79] offset:1024 sc1\n"
        "global_load_dwordx4 v[104:107], v193, s[78:79] offset:0 sc1\n"
        "global_load_dwordx4 v[108:111], v193, s[78:79] offset:1024 sc1\n"
        "global_load_dwordx4 v[112:115], v194, s[78:79] offset:0 sc1\n"
        "global_load_dwordx4 v[116:119], v194, s[78:79] offset:1024 sc1\n"
        "global_load_dwordx4 v[120:123], v195, s[78:79] offset:0 sc1\n"
        "global_load_dwordx4 v[124:127], v195, s[78:79] offset:1024 sc1\n"
        "global_load_dwordx4 v[128:131], v196, s[78:79] offset:0 sc1\n"
        "global_load_dwordx4 v[132:135], v196, s[78:79] offset:1024 sc1\n"
        "global_load_dwordx4 v[136:139], v197, s[78:79] offset:0 sc1\n"
        "global_load_dwordx4 v[140:143], v197, s[78:79] offset:1024 sc1\n"
        "global_load_dwordx4 v[144:147], v198, s[78:79] offset:0 sc1\n"
        "global_load_dwordx4 v[148:151], v198, s[78:79] offset:1024 sc1\n"
        "s_waitcnt vmcnt(24)\n"
        "ds_read_b128 a[200:203], v28 offset:10240\n"
        "ds_read_b128 a[216:219], v29 offset:10240\n"
        "s_waitcnt vmcnt(22)\n"
        "ds_read_b128 a[232:235], v28 offset:12288\n"
        "ds_read_b128 a[248:251], v29 offset:12288\n"
        "s_waitcnt vmcnt(20)\n"
        "ds_read_b128 a[140:143], v28 offset:0\n"
        "ds_read_b128 a[156:159], v29 offset:0\n"
        "s_waitcnt vmcnt(18)\n"
        "ds_read_b128 a[172:175], v28 offset:2048\n"
        "ds_read_b128 a[188:191], v29 offset:2048\n"
        "s_waitcnt vmcnt(16)\n"
        "ds_read_b128 a[204:207], v28 offset:4096\n"
        "ds_read_b128 a[220:223], v29 offset:4096\n"
        "s_waitcnt vmcnt(14)\n"
        "ds_read_b128 a[236:239], v28 offset:6144\n"
        "ds_read_b128 a[252:255], v29 offset:6144\n"
        "s_waitcnt lgkmcnt(0)\n"
        "s_waitcnt vmcnt(12)\n"
        "v_mfma_f32_16x16x32_bf16 v[56:59], v[96:99], a[0:3], 0\n"
        "global_load_dwordx4 v[152:155], v199, s[78:79] offset:0 sc1\n"
        "global_load_dwordx4 v[156:159], v199, s[78:79] offset:1024 sc1\n"
        "s_add_u32 s78, s78, 0x10080\n"
        "s_addc_u32 s79, s79, 0\n"
        "v_mfma_f32_16x16x32_bf16 v[56:59], v[100:103], a[16:19], v[56:59]\n"
        "v_mfma_f32_16x16x32_bf16 v[60:63], v[96:99], a[4:7], 0\n"
        "v_mfma_f32_16x16x32_bf16 v[60:63], v[100:103], a[20:23], v[60:63]\n"
        "v_mfma_f32_16x16x32_bf16 v[64:67], v[96:99], a[8:11], 0\n"
        "v_mfma_f32_16x16x32_bf16 v[64:67], v[100:103], a[24:27], v[64:67]\n"
        "v_mfma_f32_16x16x32_bf16 v[68:71], v[96:99], a[12:15], 0\n"
        "v_mfma_f32_16x16x32_bf16 v[68:71], v[100:103], a[28:31], v[68:71]\n"
        "s_waitcnt vmcnt(12)\n"
        "v_mfma_f32_16x16x32_bf16 v[56:59], v[104:107], a[32:35], v[56:59]\n"
        "global_load_dwordx4 v[96:99], v192, s[78:79] offset:0 sc1\n"
        "global_load_dwordx4 v[100:103], v192, s[78:79] offset:1024 sc1\n"
        "v_mfma_f32_16x16x32_bf16 v[56:59], v[108:111], a[48:51], v[56:59]\n"
        "v_mfma_f32_16x16x32_bf16 v[60:63], v[104:107], a[36:39], v[60:63]\n"
        "v_mfma_f32_16x16x32_bf16 v[60:63], v[108:111], a[52:55], v[60:63]\n"
        "v_mfma_f32_16x16x32_bf16 v[64:67], v[104:107], a[40:43], v[64:67]\n"
        "v_mfma_f32_16x16x32_bf16 v[64:67], v[108:111], a[56:59], v[64:67]\n"
        "v_mfma_f32_16x16x32_bf16 v[68:71], v[104:107], a[44:47], v[68:71]\n"
        "v_mfma_f32_16x16x32_bf16 v[68:71], v[108:111], a[60:63], v[68:71]\n"
        "s_waitcnt vmcnt(12)\n"
        "v_mfma_f32_16x16x32_bf16 v[56:59], v[112:115], a[64:67], v[56:59]\n"
        "global_load_dwordx4 v[104:107], v193, s[78:79] offset:0 sc1\n"
        "global_load_dwordx4 v[108:111], v193, s[78:79] offset:1024 sc1\n"
        "v_mfma_f32_16x16x32_bf16 v[56:59], v[116:119], a[80:83], v[56:59]\n"
        "v_mfma_f32_16x16x32_bf16 v[60:63], v[112:115], a[68:71], v[60:63]\n"
        "v_mfma_f32_16x16x32_bf16 v[60:63], v[116:119], a[84:87], v[60:63]\n"
        "v_mfma_f32_16x16x32_bf16 v[64:67], v[112:115], a[72:75], v[64:67]\n"
        "v_mfma_f32_16x16x32_bf16 v[64:67], v[116:119], a[88:91], v[64:67]\n"
        "v_mfma_f32_16x16x32_bf16 v[68:71], v[112:115], a[76:79], v[68:71]\n"
        "v_mfma_f32_16x16x32_bf16 v[68:71], v[116:119], a[92:95], v[68:71]\n"
        "s_waitcnt vmcnt(12)\n"
        "v_mfma_f32_16x16x32_bf16 v[56:59], v[120:123], a[96:99], v[56:59]\n"
        "global_load_dwordx4 v[112:115], v194, s[78:79] offset:0 sc1\n"
        "global_load_dwordx4 v[116:119], v194, s[78:79] offset:1024 sc1\n"
        "v_mfma_f32_16x16x32_bf16 v[56:59], v[124:127], a[112:115], v[56:59]\n"
        "v_mfma_f32_16x16x32_bf16 v[60:63], v[120:123], a[100:103], v[60:63]\n"
        "v_mfma_f32_16x16x32_bf16 v[60:63], v[124:127], a[116:119], v[60:63]\n"
        "v_mfma_f32_16x16x32_bf16 v[64:67], v[120:123], a[104:107], v[64:67]\n"
        "v_mfma_f32_16x16x32_bf16 v[64:67], v[124:127], a[120:123], v[64:67]\n"
        "v_mfma_f32_16x16x32_bf16 v[68:71], v[120:123], a[108:111], v[68:71]\n"
        "v_mfma_f32_16x16x32_bf16 v[68:71], v[124:127], a[124:127], v[68:71]\n"
        "s_waitcnt vmcnt(12)\n"
        "v_mfma_f32_16x16x32_bf16 v[56:59], v[128:131], a[128:131], v[56:59]\n"
        "global_load_dwordx4 v[120:123], v195, s[78:79] offset:0 sc1\n"
        "global_load_dwordx4 v[124:127], v195, s[78:79] offset:1024 sc1\n"
        "v_mfma_f32_16x16x32_bf16 v[56:59], v[132:135], a[144:147], v[56:59]\n"
        "v_lshrrev_b32 v38, 4, v1\n"
        "v_lshlrev_b32 v38, 3, v38\n"
        "v_mfma_f32_16x16x32_bf16 v[60:63], v[128:131], a[132:135], v[60:63]\n"
        "v_sub_u32 v38, v52, v38\n"
        "v_mfma_f32_16x16x32_bf16 v[60:63], v[132:135], a[148:151], v[60:63]\n"
        "v_mfma_f32_16x16x32_bf16 v[64:67], v[128:131], a[136:139], v[64:67]\n"
        "v_mfma_f32_16x16x32_bf16 v[64:67], v[132:135], a[152:155], v[64:67]\n"
        "v_mfma_f32_16x16x32_bf16 v[68:71], v[128:131], a[140:143], v[68:71]\n"
        "v_mfma_f32_16x16x32_bf16 v[68:71], v[132:135], a[156:159], v[68:71]\n"
        "s_waitcnt vmcnt(12)\n"
        "v_mfma_f32_16x16x32_bf16 v[56:59], v[136:139], a[160:163], v[56:59]\n"
        "global_load_dwordx4 v[128:131], v196, s[78:79] offset:0 sc1\n"
        "global_load_dwordx4 v[132:135], v196, s[78:79] offset:1024 sc1\n"
        "v_mfma_f32_16x16x32_bf16 v[56:59], v[140:143], a[176:179], v[56:59]\n"
        "v_mfma_f32_16x16x32_bf16 v[60:63], v[136:139], a[164:167], v[60:63]\n"
        "v_mfma_f32_16x16x32_bf16 v[60:63], v[140:143], a[180:183], v[60:63]\n"
        "v_mfma_f32_16x16x32_bf16 v[64:67], v[136:139], a[168:171], v[64:67]\n"
        "v_mfma_f32_16x16x32_bf16 v[64:67], v[140:143], a[184:187], v[64:67]\n"
        "v_mfma_f32_16x16x32_bf16 v[68:71], v[136:139], a[172:175], v[68:71]\n"
        "v_mfma_f32_16x16x32_bf16 v[68:71], v[140:143], a[188:191], v[68:71]\n"
        "s_waitcnt vmcnt(12)\n"
        "v_mfma_f32_16x16x32_bf16 v[56:59], v[144:147], a[192:195], v[56:59]\n"
        "global_load_dwordx4 v[136:139], v197, s[78:79] offset:0 sc1\n"
        "global_load_dwordx4 v[140:143], v197, s[78:79] offset:1024 sc1\n"
        "v_mfma_f32_16x16x32_bf16 v[56:59], v[148:151], a[208:211], v[56:59]\n"
        "v_mfma_f32_16x16x32_bf16 v[60:63], v[144:147], a[196:199], v[60:63]\n"
        "v_mfma_f32_16x16x32_bf16 v[60:63], v[148:151], a[212:215], v[60:63]\n"
        "v_mfma_f32_16x16x32_bf16 v[64:67], v[144:147], a[200:203], v[64:67]\n"
        "v_mfma_f32_16x16x32_bf16 v[64:67], v[148:151], a[216:219], v[64:67]\n"
        "v_mfma_f32_16x16x32_bf16 v[68:71], v[144:147], a[204:207], v[68:71]\n"
        "v_mfma_f32_16x16x32_bf16 v[68:71], v[148:151], a[220:223], v[68:71]\n"
        "s_waitcnt vmcnt(12)\n"
        "v_mfma_f32_16x16x32_bf16 v[56:59], v[152:155], a[224:227], v[56:59]\n"
        "global_load_dwordx4 v[144:147], v198, s[78:79] offset:0 sc1\n"
        "global_load_dwordx4 v[148:151], v198, s[78:79] offset:1024 sc1\n"
        "v_mfma_f32_16x16x32_bf16 v[56:59], v[156:159], a[240:243], v[56:59]\n"
        "v_mfma_f32_16x16x32_bf16 v[60:63], v[152:155], a[228:231], v[60:63]\n"
        "v_mfma_f32_16x16x32_bf16 v[60:63], v[156:159], a[244:247], v[60:63]\n"
        "v_mfma_f32_16x16x32_bf16 v[64:67], v[152:155], a[232:235], v[64:67]\n"
        "v_mfma_f32_16x16x32_bf16 v[64:67], v[156:159], a[248:251], v[64:67]\n"
        "v_mfma_f32_16x16x32_bf16 v[68:71], v[152:155], a[236:239], v[68:71]\n"
        "v_mfma_f32_16x16x32_bf16 v[68:71], v[156:159], a[252:255], v[68:71]\n"
        "s_waitcnt vmcnt(12)\n"
        "v_mfma_f32_16x16x32_bf16 v[72:75], v[96:99], a[0:3], 0\n"
        "global_load_dwordx4 v[152:155], v199, s[78:79] offset:0 sc1\n"
        "global_load_dwordx4 v[156:159], v199, s[78:79] offset:1024 sc1\n"
        "s_add_u32 s78, s78, 0x10080\n"
        "s_addc_u32 s79, s79, 0\n"
        "v_mfma_f32_16x16x32_bf16 v[72:75], v[100:103], a[16:19], v[72:75]\n"
        "v_mfma_f32_16x16x32_bf16 v[76:79], v[96:99], a[4:7], 0\n"
        "v_mfma_f32_16x16x32_bf16 v[76:79], v[100:103], a[20:23], v[76:79]\n"
        "v_mfma_f32_16x16x32_bf16 v[80:83], v[96:99], a[8:11], 0\n"
        "ds_write_b128 v47, v[60:63] offset:0\n"
        "ds_write_b128 v47, v[64:67] offset:1024\n"
        "v_mfma_f32_16x16x32_bf16 v[80:83], v[100:103], a[24:27], v[80:83]\n"
        "ds_write_b128 v47, v[68:71] offset:2048\n"
        "v_mfma_f32_16x16x32_bf16 v[84:87], v[96:99], a[12:15], 0\n"
        "v_mfma_f32_16x16x32_bf16 v[84:87], v[100:103], a[28:31], v[84:87]\n"
        "s_waitcnt vmcnt(12)\n"
        "v_mfma_f32_16x16x32_bf16 v[72:75], v[104:107], a[32:35], v[72:75]\n"
        "global_load_dwordx4 v[96:99], v192, s[78:79] offset:0 sc1\n"
        "global_load_dwordx4 v[100:103], v192, s[78:79] offset:1024 sc1\n"
        "v_mfma_f32_16x16x32_bf16 v[72:75], v[108:111], a[48:51], v[72:75]\n"
        "v_mfma_f32_16x16x32_bf16 v[76:79], v[104:107], a[36:39], v[76:79]\n"
        "v_mfma_f32_16x16x32_bf16 v[76:79], v[108:111], a[52:55], v[76:79]\n"
        "v_mfma_f32_16x16x32_bf16 v[80:83], v[104:107], a[40:43], v[80:83]\n"
        "s_waitcnt lgkmcnt(0)\n"
        "s_barrier\n"
        "v_mfma_f32_16x16x32_bf16 v[80:83], v[108:111], a[56:59], v[80:83]\n"
        "ds_read_b128 v[4:7], v48 offset:0\n"
        "ds_read_b128 v[8:11], v49 offset:0\n"
        "v_mfma_f32_16x16x32_bf16 v[84:87], v[104:107], a[44:47], v[84:87]\n"
        "ds_read_b128 v[12:15], v50 offset:0\n"
        "v_mfma_f32_16x16x32_bf16 v[84:87], v[108:111], a[60:63], v[84:87]\n"
        "s_waitcnt vmcnt(12)\n"
        "v_mfma_f32_16x16x32_bf16 v[72:75], v[112:115], a[64:67], v[72:75]\n"
        "global_load_dwordx4 v[104:107], v193, s[78:79] offset:0 sc1\n"
        "global_load_dwordx4 v[108:111], v193, s[78:79] offset:1024 sc1\n"
        "v_mfma_f32_16x16x32_bf16 v[72:75], v[116:119], a[80:83], v[72:75]\n"
        "global_load_dword v36, v32, s[30:31] sc1\n"
        "v_mfma_f32_16x16x32_bf16 v[76:79], v[112:115], a[68:71], v[76:79]\n"
        "v_mfma_f32_16x16x32_bf16 v[76:79], v[116:119], a[84:87], v[76:79]\n"
        "v_mfma_f32_16x16x32_bf16 v[80:83], v[112:115], a[72:75], v[80:83]\n"
        "s_waitcnt lgkmcnt(0)\n"
        "v_add_f32 v4, v4, v8\n"
        "v_mfma_f32_16x16x32_bf16 v[80:83], v[116:119], a[88:91], v[80:83]\n"
        "v_add_f32 v5, v5, v9\n"
        "v_add_f32 v6, v6, v10\n"
        "v_mfma_f32_16x16x32_bf16 v[84:87], v[112:115], a[76:79], v[84:87]\n"
        "v_add_f32 v7, v7, v11\n"
        "v_add_f32 v12, v56, v12\n"
        "v_mfma_f32_16x16x32_bf16 v[84:87], v[116:119], a[92:95], v[84:87]\n"
        "v_add_f32 v13, v57, v13\n"
        "s_waitcnt vmcnt(13)\n"
        "v_mfma_f32_16x16x32_bf16 v[72:75], v[120:123], a[96:99], v[72:75]\n"
        "global_load_dwordx4 v[112:115], v194, s[78:79] offset:0 sc1\n"
        "global_load_dwordx4 v[116:119], v194, s[78:79] offset:1024 sc1\n"
        "v_mfma_f32_16x16x32_bf16 v[72:75], v[124:127], a[112:115], v[72:75]\n"
        "v_add_f32 v14, v58, v14\n"
        "v_add_f32 v15, v59, v15\n"
        "v_mfma_f32_16x16x32_bf16 v[76:79], v[120:123], a[100:103], v[76:79]\n"
        "v_add_f32 v12, v12, v4\n"
        "v_add_f32 v13, v13, v5\n"
        "v_mfma_f32_16x16x32_bf16 v[76:79], v[124:127], a[116:119], v[76:79]\n"
        "v_add_f32 v14, v14, v6\n"
        "v_add_f32 v15, v15, v7\n"
        "v_mfma_f32_16x16x32_bf16 v[80:83], v[120:123], a[104:107], v[80:83]\n"
        "v_cvt_pk_bf16_f32 v228, v12, v13\n"
        "v_cvt_pk_bf16_f32 v229, v14, v15\n"
        "v_mfma_f32_16x16x32_bf16 v[80:83], v[124:127], a[120:123], v[80:83]\n"
        "v_min_u32 v228, -2, v228\n"
        "v_min_u32 v229, -2, v229\n"
        "v_mfma_f32_16x16x32_bf16 v[84:87], v[120:123], a[108:111], v[84:87]\n"
        "v_mfma_f32_16x16x32_bf16 v[84:87], v[124:127], a[124:127], v[84:87]\n"
        "s_waitcnt vmcnt(13)\n"
        "v_mfma_f32_16x16x32_bf16 v[72:75], v[128:131], a[128:131], v[72:75]\n"
        "global_load_dwordx4 v[120:123], v195, s[78:79] offset:0 sc1\n"
        "global_load_dwordx4 v[124:127], v195, s[78:79] offset:1024 sc1\n"
        "v_mfma_f32_16x16x32_bf16 v[72:75], v[132:135], a[144:147], v[72:75]\n"
        "v_mfma_f32_16x16x32_bf16 v[76:79], v[128:131], a[132:135], v[76:79]\n"
        "v_mfma_f32_16x16x32_bf16 v[76:79], v[132:135], a[148:151], v[76:79]\n"
        "v_mfma_f32_16x16x32_bf16 v[80:83], v[128:131], a[136:139], v[80:83]\n"
        "v_mfma_f32_16x16x32_bf16 v[80:83], v[132:135], a[152:155], v[80:83]\n"
        "v_mfma_f32_16x16x32_bf16 v[84:87], v[128:131], a[140:143], v[84:87]\n"
        "v_mfma_f32_16x16x32_bf16 v[84:87], v[132:135], a[156:159], v[84:87]\n"
        "s_waitcnt vmcnt(13)\n"
        "v_mfma_f32_16x16x32_bf16 v[72:75], v[136:139], a[160:163], v[72:75]\n"
        "global_load_dwordx4 v[128:131], v196, s[78:79] offset:0 sc1\n"
        "global_load_dwordx4 v[132:135], v196, s[78:79] offset:1024 sc1\n"
        "v_mfma_f32_16x16x32_bf16 v[72:75], v[140:143], a[176:179], v[72:75]\n"
        "v_mfma_f32_16x16x32_bf16 v[76:79], v[136:139], a[164:167], v[76:79]\n"
        "v_mfma_f32_16x16x32_bf16 v[76:79], v[140:143], a[180:183], v[76:79]\n"
        "v_mfma_f32_16x16x32_bf16 v[80:83], v[136:139], a[168:171], v[80:83]\n"
        "v_mfma_f32_16x16x32_bf16 v[80:83], v[140:143], a[184:187], v[80:83]\n"
        "v_mfma_f32_16x16x32_bf16 v[84:87], v[136:139], a[172:175], v[84:87]\n"
        "v_mfma_f32_16x16x32_bf16 v[84:87], v[140:143], a[188:191], v[84:87]\n"
        "s_waitcnt vmcnt(13)\n"
        "v_mfma_f32_16x16x32_bf16 v[72:75], v[144:147], a[192:195], v[72:75]\n"
        "global_load_dwordx4 v[136:139], v197, s[78:79] offset:0 sc1\n"
        "global_load_dwordx4 v[140:143], v197, s[78:79] offset:1024 sc1\n"
        "v_mfma_f32_16x16x32_bf16 v[72:75], v[148:151], a[208:211], v[72:75]\n"
        "v_mfma_f32_16x16x32_bf16 v[76:79], v[144:147], a[196:199], v[76:79]\n"
        "v_mfma_f32_16x16x32_bf16 v[76:79], v[148:151], a[212:215], v[76:79]\n"
        "v_mfma_f32_16x16x32_bf16 v[80:83], v[144:147], a[200:203], v[80:83]\n"
        "v_mfma_f32_16x16x32_bf16 v[80:83], v[148:151], a[216:219], v[80:83]\n"
        "v_mfma_f32_16x16x32_bf16 v[84:87], v[144:147], a[204:207], v[84:87]\n"
        "v_mfma_f32_16x16x32_bf16 v[84:87], v[148:151], a[220:223], v[84:87]\n"
        "s_waitcnt vmcnt(13)\n"
        "v_mfma_f32_16x16x32_bf16 v[72:75], v[152:155], a[224:227], v[72:75]\n"
        "global_load_dwordx4 v[144:147], v198, s[78:79] offset:0 sc1\n"
        "global_load_dwordx4 v[148:151], v198, s[78:79] offset:1024 sc1\n"
        "v_mfma_f32_16x16x32_bf16 v[72:75], v[156:159], a[240:243], v[72:75]\n"
        "v_mfma_f32_16x16x32_bf16 v[76:79], v[152:155], a[228:231], v[76:79]\n"
        "v_mfma_f32_16x16x32_bf16 v[76:79], v[156:159], a[244:247], v[76:79]\n"
        "v_mfma_f32_16x16x32_bf16 v[80:83], v[152:155], a[232:235], v[80:83]\n"
        "v_mfma_f32_16x16x32_bf16 v[80:83], v[156:159], a[248:251], v[80:83]\n"
        "v_mfma_f32_16x16x32_bf16 v[84:87], v[152:155], a[236:239], v[84:87]\n"
        "v_mfma_f32_16x16x32_bf16 v[84:87], v[156:159], a[252:255], v[84:87]\n"
        "s_waitcnt vmcnt(13)\n"
        "v_mfma_f32_16x16x32_bf16 v[56:59], v[96:99], a[0:3], 0\n"
        "global_load_dwordx4 v[152:155], v199, s[78:79] offset:0 sc1\n"
        "global_load_dwordx4 v[156:159], v199, s[78:79] offset:1024 sc1\n"
        "s_add_u32 s78, s78, 0x10080\n"
        "s_addc_u32 s79, s79, 0\n"
        "v_mfma_f32_16x16x32_bf16 v[56:59], v[100:103], a[16:19], v[56:59]\n"
        "v_mfma_f32_16x16x32_bf16 v[60:63], v[96:99], a[4:7], 0\n"
        "v_mfma_f32_16x16x32_bf16 v[60:63], v[100:103], a[20:23], v[60:63]\n"
        "v_mfma_f32_16x16x32_bf16 v[64:67], v[96:99], a[8:11], 0\n"
        "ds_write_b128 v47, v[76:79] offset:3072\n"
        "ds_write_b128 v47, v[80:83] offset:4096\n"
        "v_mfma_f32_16x16x32_bf16 v[64:67], v[100:103], a[24:27], v[64:67]\n"
        "ds_write_b128 v47, v[84:87] offset:5120\n"
        "v_mfma_f32_16x16x32_bf16 v[68:71], v[96:99], a[12:15], 0\n"
        "v_mfma_f32_16x16x32_bf16 v[68:71], v[100:103], a[28:31], v[68:71]\n"
        "s_waitcnt vmcnt(13)\n"
        "v_mfma_f32_16x16x32_bf16 v[56:59], v[104:107], a[32:35], v[56:59]\n"
        "global_load_dwordx4 v[96:99], v192, s[78:79] offset:0 sc1\n"
        "global_load_dwordx4 v[100:103], v192, s[78:79] offset:1024 sc1\n"
        "v_mfma_f32_16x16x32_bf16 v[56:59], v[108:111], a[48:51], v[56:59]\n"
        "v_mfma_f32_16x16x32_bf16 v[60:63], v[104:107], a[36:39], v[60:63]\n"
        "v_mfma_f32_16x16x32_bf16 v[60:63], v[108:111], a[52:55], v[60:63]\n"
        "v_mfma_f32_16x16x32_bf16 v[64:67], v[104:107], a[40:43], v[64:67]\n"
        "s_waitcnt lgkmcnt(0)\n"
        "s_barrier\n"
        "v_mfma_f32_16x16x32_bf16 v[64:67], v[108:111], a[56:59], v[64:67]\n"
        "ds_read_b128 v[4:7], v48 offset:3072\n"
        "ds_read_b128 v[8:11], v49 offset:3072\n"
        "v_mfma_f32_16x16x32_bf16 v[68:71], v[104:107], a[44:47], v[68:71]\n"
        "ds_read_b128 v[12:15], v50 offset:3072\n"
        "v_mfma_f32_16x16x32_bf16 v[68:71], v[108:111], a[60:63], v[68:71]\n"
        "s_waitcnt vmcnt(12)\n"
        "v_mfma_f32_16x16x32_bf16 v[56:59], v[112:115], a[64:67], v[56:59]\n"
        "global_load_dwordx4 v[104:107], v193, s[78:79] offset:0 sc1\n"
        "global_load_dwordx4 v[108:111], v193, s[78:79] offset:1024 sc1\n"
        "v_mfma_f32_16x16x32_bf16 v[56:59], v[116:119], a[80:83], v[56:59]\n"
        "v_mfma_f32_16x16x32_bf16 v[60:63], v[112:115], a[68:71], v[60:63]\n"
        "v_mfma_f32_16x16x32_bf16 v[60:63], v[116:119], a[84:87], v[60:63]\n"
        "v_mfma_f32_16x16x32_bf16 v[64:67], v[112:115], a[72:75], v[64:67]\n"
        "s_waitcnt lgkmcnt(0)\n"
        "v_add_f32 v4, v4, v8\n"
        "v_mfma_f32_16x16x32_bf16 v[64:67], v[116:119], a[88:91], v[64:67]\n"
        "v_add_f32 v5, v5, v9\n"
        "v_add_f32 v6, v6, v10\n"
        "v_mfma_f32_16x16x32_bf16 v[68:71], v[112:115], a[76:79], v[68:71]\n"
        "v_add_f32 v7, v7, v11\n"
        "v_add_f32 v12, v72, v12\n"
        "v_mfma_f32_16x16x32_bf16 v[68:71], v[116:119], a[92:95], v[68:71]\n"
        "v_add_f32 v13, v73, v13\n"
        "s_waitcnt vmcnt(12)\n"
        "v_mfma_f32_16x16x32_bf16 v[56:59], v[120:123], a[96:99], v[56:59]\n"
        "global_load_dwordx4 v[112:115], v194, s[78:79] offset:0 sc1\n"
        "global_load_dwordx4 v[116:119], v194, s[78:79] offset:1024 sc1\n"
        "v_mfma_f32_16x16x32_bf16 v[56:59], v[124:127], a[112:115], v[56:59]\n"
        "v_add_f32 v14, v74, v14\n"
        "v_add_f32 v15, v75, v15\n"
        "v_mfma_f32_16x16x32_bf16 v[60:63], v[120:123], a[100:103], v[60:63]\n"
        "v_add_f32 v12, v12, v4\n"
        "v_add_f32 v13, v13, v5\n"
        "v_mfma_f32_16x16x32_bf16 v[60:63], v[124:127], a[116:119], v[60:63]\n"
        "v_add_f32 v14, v14, v6\n"
        "v_add_f32 v15, v15, v7\n"
        "v_cmp_ne_u32 vcc, v36, v33\n"
        "s_nop 4\n"
        "s_cbranch_vccz Lrc_17\n"
        "v_mov_b32 v33, v36\n"
        "s_cmp_eq_u32 s43, 0\n"
        "s_cbranch_scc0 Lw_16\n"
        "s_mov_b64 exec, s[22:23]\n"
        "global_store_dword v34, v33, s[30:31] sc1\n"
        "s_mov_b64 exec, -1\n"
        "Lw_16:\n"
        "s_waitcnt vmcnt(0)\n"
        "Lrc_17:\n"
        "v_mfma_f32_16x16x32_bf16 v[64:67], v[120:123], a[104:107], v[64:67]\n"
        "v_cvt_pk_bf16_f32 v230, v12, v13\n"
        "v_cvt_pk_bf16_f32 v231, v14, v15\n"
        "v_mfma_f32_16x16x32_bf16 v[64:67], v[124:127], a[120:123], v[64:67]\n"
        "v_min_u32 v230, -2, v230\n"
        "v_min_u32 v231, -2, v231\n"
        "v_mfma_f32_16x16x32_bf16 v[68:71], v[120:123], a[108:111], v[68:71]\n"
        "v_mfma_f32_16x16x32_bf16 v[68:71], v[124:127], a[124:127], v[68:71]\n"
        "s_waitcnt vmcnt(12)\n"
        "v_mfma_f32_16x16x32_bf16 v[56:59], v[128:131], a[128:131], v[56:59]\n"
        "global_load_dwordx4 v[120:123], v195, s[78:79] offset:0 sc1\n"
        "global_load_dwordx4 v[124:127], v195, s[78:79] offset:1024 sc1\n"
        "v_mfma_f32_16x16x32_bf16 v[56:59], v[132:135], a[144:147], v[56:59]\n"
        "v_mfma_f32_16x16x32_bf16 v[60:63], v[128:131], a[132:135], v[60:63]\n"
        "v_mfma_f32_16x16x32_bf16 v[60:63], v[132:135], a[148:151], v[60:63]\n"
        "v_mfma_f32_16x16x32_bf16 v[64:67], v[128:131], a[136:139], v[64:67]\n"
        "v_mfma_f32_16x16x32_bf16 v[64:67], v[132:135], a[152:155], v[64:67]\n"
        "v_mfma_f32_16x16x32_bf16 v[68:71], v[128:131], a[140:143], v[68:71]\n"
        "v_mfma_f32_16x16x32_bf16 v[68:71], v[132:135], a[156:159], v[68:71]\n"
        "s_waitcnt vmcnt(12)\n"
        "v_mfma_f32_16x16x32_bf16 v[56:59], v[136:139], a[160:163], v[56:59]\n"
        "global_load_dwordx4 v[128:131], v196, s[78:79] offset:0 sc1\n"
        "global_load_dwordx4 v[132:135], v196, s[78:79] offset:1024 sc1\n"
        "v_mfma_f32_16x16x32_bf16 v[56:59], v[140:143], a[176:179], v[56:59]\n"
        "v_mfma_f32_16x16x32_bf16 v[60:63], v[136:139], a[164:167], v[60:63]\n"
        "v_mfma_f32_16x16x32_bf16 v[60:63], v[140:143], a[180:183], v[60:63]\n"
        "v_mfma_f32_16x16x32_bf16 v[64:67], v[136:139], a[168:171], v[64:67]\n"
        "v_mfma_f32_16x16x32_bf16 v[64:67], v[140:143], a[184:187], v[64:67]\n"
        "v_mfma_f32_16x16x32_bf16 v[68:71], v[136:139], a[172:175], v[68:71]\n"
        "v_mfma_f32_16x16x32_bf16 v[68:71], v[140:143], a[188:191], v[68:71]\n"
        "s_waitcnt vmcnt(12)\n"
        "v_mfma_f32_16x16x32_bf16 v[56:59], v[144:147], a[192:195], v[56:59]\n"
        "global_load_dwordx4 v[136:139], v197, s[78:79] offset:0 sc1\n"
        "global_load_dwordx4 v[140:143], v197, s[78:79] offset:1024 sc1\n"
        "v_mfma_f32_16x16x32_bf16 v[56:59], v[148:151], a[208:211], v[56:59]\n"
        "v_mfma_f32_16x16x32_bf16 v[60:63], v[144:147], a[196:199], v[60:63]\n"
        "v_mfma_f32_16x16x32_bf16 v[60:63], v[148:151], a[212:215], v[60:63]\n"
        "v_mfma_f32_16x16x32_bf16 v[64:67], v[144:147], a[200:203], v[64:67]\n"
        "v_mfma_f32_16x16x32_bf16 v[64:67], v[148:151], a[216:219], v[64:67]\n"
        "v_mfma_f32_16x16x32_bf16 v[68:71], v[144:147], a[204:207], v[68:71]\n"
        "v_mfma_f32_16x16x32_bf16 v[68:71], v[148:151], a[220:223], v[68:71]\n"
        "s_waitcnt vmcnt(12)\n"
        "v_mfma_f32_16x16x32_bf16 v[56:59], v[152:155], a[224:227], v[56:59]\n"
        "global_load_dwordx4 v[144:147], v198, s[78:79] offset:0 sc1\n"
        "global_load_dwordx4 v[148:151], v198, s[78:79] offset:1024 sc1\n"
        "v_mfma_f32_16x16x32_bf16 v[56:59], v[156:159], a[240:243], v[56:59]\n"
        "v_mfma_f32_16x16x32_bf16 v[60:63], v[152:155], a[228:231], v[60:63]\n"
        "v_mfma_f32_16x16x32_bf16 v[60:63], v[156:159], a[244:247], v[60:63]\n"
        "v_mfma_f32_16x16x32_bf16 v[64:67], v[152:155], a[232:235], v[64:67]\n"
        "v_mfma_f32_16x16x32_bf16 v[64:67], v[156:159], a[248:251], v[64:67]\n"
        "v_mfma_f32_16x16x32_bf16 v[68:71], v[152:155], a[236:239], v[68:71]\n"
        "v_mfma_f32_16x16x32_bf16 v[68:71], v[156:159], a[252:255], v[68:71]\n"
        "s_waitcnt vmcnt(12)\n"
        "v_mfma_f32_16x16x32_bf16 v[72:75], v[96:99], a[0:3], 0\n"
        "global_load_dwordx4 v[152:155], v199, s[78:79] offset:0 sc1\n"
        "global_load_dwordx4 v[156:159], v199, s[78:79] offset:1024 sc1\n"
        "s_sub_u32 s78, s78, 0x80400\n"
        "s_subb_u32 s79, s79, 0\n"
        "v_mfma_f32_16x16x32_bf16 v[72:75], v[100:103], a[16:19], v[72:75]\n"
        "v_mfma_f32_16x16x32_bf16 v[76:79], v[96:99], a[4:7], 0\n"
        "v_mfma_f32_16x16x32_bf16 v[76:79], v[100:103], a[20:23], v[76:79]\n"
        "v_mfma_f32_16x16x32_bf16 v[80:83], v[96:99], a[8:11], 0\n"
        "ds_write_b128 v47, v[60:63] offset:0\n"
        "ds_write_b128 v47, v[64:67] offset:1024\n"
        "v_mfma_f32_16x16x32_bf16 v[80:83], v[100:103], a[24:27], v[80:83]\n"
        "ds_write_b128 v47, v[68:71] offset:2048\n"
        "v_mfma_f32_16x16x32_bf16 v[84:87], v[96:99], a[12:15], 0\n"
        "v_mfma_f32_16x16x32_bf16 v[84:87], v[100:103], a[28:31], v[84:87]\n"
        "s_waitcnt vmcnt(12)\n"
        "v_mfma_f32_16x16x32_bf16 v[72:75], v[104:107], a[32:35], v[72:75]\n"
        "global_load_dwordx4 v[96:99], v192, s[78:79] offset:0 sc1\n"
        "global_load_dwordx4 v[100:103], v192, s[78:79] offset:1024 sc1\n"
        "v_mfma_f32_16x16x32_bf16 v[72:75], v[108:111], a[48:51], v[72:75]\n"
        "v_mfma_f32_16x16x32_bf16 v[76:79], v[104:107], a[36:39], v[76:79]\n"
        "v_mfma_f32_16x16x32_bf16 v[76:79], v[108:111], a[52:55], v[76:79]\n"
        "v_mfma_f32_16x16x32_bf16 v[80:83], v[104:107], a[40:43], v[80:83]\n"
        "s_waitcnt lgkmcnt(0)\n"
        "s_barrier\n"
        "v_mfma_f32_16x16x32_bf16 v[80:83], v[108:111], a[56:59], v[80:83]\n"
        "ds_read_b128 v[4:7], v48 offset:0\n"
        "ds_read_b128 v[8:11], v49 offset:0\n"
        "v_mfma_f32_16x16x32_bf16 v[84:87], v[104:107], a[44:47], v[84:87]\n"
        "ds_read_b128 v[12:15], v50 offset:0\n"
        "v_mfma_f32_16x16x32_bf16 v[84:87], v[108:111], a[60:63], v[84:87]\n"
        "s_waitcnt vmcnt(12)\n"
        "v_mfma_f32_16x16x32_bf16 v[72:75], v[112:115], a[64:67], v[72:75]\n"
        "global_load_dwordx4 v[104:107], v193, s[78:79] offset:0 sc1\n"
        "global_load_dwordx4 v[108:111], v193, s[78:79] offset:1024 sc1\n"
        "v_mfma_f32_16x16x32_bf16 v[72:75], v[116:119], a[80:83], v[72:75]\n"
        "v_mfma_f32_16x16x32_bf16 v[76:79], v[112:115], a[68:71], v[76:79]\n"
        "v_mfma_f32_16x16x32_bf16 v[76:79], v[116:119], a[84:87], v[76:79]\n"
        "v_mfma_f32_16x16x32_bf16 v[80:83], v[112:115], a[72:75], v[80:83]\n"
        "s_waitcnt lgkmcnt(0)\n"
        "v_add_f32 v4, v4, v8\n"
        "v_mfma_f32_16x16x32_bf16 v[80:83], v[116:119], a[88:91], v[80:83]\n"
        "v_add_f32 v5, v5, v9\n"
        "v_add_f32 v6, v6, v10\n"
        "v_mfma_f32_16x16x32_bf16 v[84:87], v[112:115], a[76:79], v[84:87]\n"
        "v_add_f32 v7, v7, v11\n"
        "v_add_f32 v12, v56, v12\n"
        "v_mfma_f32_16x16x32_bf16 v[84:87], v[116:119], a[92:95], v[84:87]\n"
        "v_add_f32 v13, v57, v13\n"
        "s_waitcnt vmcnt(12)\n"
        "v_mfma_f32_16x16x32_bf16 v[72:75], v[120:123], a[96:99], v[72:75]\n"
        "global_load_dwordx4 v[112:115], v194, s[78:79] offset:0 sc1\n"
        "global_load_dwordx4 v[116:119], v194, s[78:79] offset:1024 sc1\n"
        "v_mfma_f32_16x16x32_bf16 v[72:75], v[124:127], a[112:115], v[72:75]\n"
        "v_add_f32 v14, v58, v14\n"
        "v_add_f32 v15, v59, v15\n"
        "v_mfma_f32_16x16x32_bf16 v[76:79], v[120:123], a[100:103], v[76:79]\n"
        "v_add_f32 v12, v12, v4\n"
        "v_add_f32 v13, v13, v5\n"
        "v_mfma_f32_16x16x32_bf16 v[76:79], v[124:127], a[116:119], v[76:79]\n"
        "v_add_f32 v14, v14, v6\n"
        "v_add_f32 v15, v15, v7\n"
        "v_mfma_f32_16x16x32_bf16 v[80:83], v[120:123], a[104:107], v[80:83]\n"
        "v_cvt_pk_bf16_f32 v232, v12, v13\n"
        "v_cvt_pk_bf16_f32 v233, v14, v15\n"
        "v_mfma_f32_16x16x32_bf16 v[80:83], v[124:127], a[120:123], v[80:83]\n"
        "v_min_u32 v232, -2, v232\n"
        "v_min_u32 v233, -2, v233\n"
        "v_mfma_f32_16x16x32_bf16 v[84:87], v[120:123], a[108:111], v[84:87]\n"
        "v_mfma_f32_16x16x32_bf16 v[84:87], v[124:127], a[124:127], v[84:87]\n"
        "s_waitcnt vmcnt(12)\n"
        "v_mfma_f32_16x16x32_bf16 v[72:75], v[128:131], a[128:131], v[72:75]\n"
        "global_load_dwordx4 v[120:123], v195, s[78:79] offset:0 sc1\n"
        "global_load_dwordx4 v[124:127], v195, s[78:79] offset:1024 sc1\n"
        "v_mfma_f32_16x16x32_bf16 v[72:75], v[132:135], a[144:147], v[72:75]\n"
        "v_mfma_f32_16x16x32_bf16 v[76:79], v[128:131], a[132:135], v[76:79]\n"
        "v_mfma_f32_16x16x32_bf16 v[76:79], v[132:135], a[148:151], v[76:79]\n"
        "v_mfma_f32_16x16x32_bf16 v[80:83], v[128:131], a[136:139], v[80:83]\n"
        "v_mfma_f32_16x16x32_bf16 v[80:83], v[132:135], a[152:155], v[80:83]\n"
        "v_mfma_f32_16x16x32_bf16 v[84:87], v[128:131], a[140:143], v[84:87]\n"
        "v_mfma_f32_16x16x32_bf16 v[84:87], v[132:135], a[156:159], v[84:87]\n"
        "s_waitcnt vmcnt(12)\n"
        "v_mfma_f32_16x16x32_bf16 v[72:75], v[136:139], a[160:163], v[72:75]\n"
        "global_load_dwordx4 v[128:131], v196, s[78:79] offset:0 sc1\n"
        "global_load_dwordx4 v[132:135], v196, s[78:79] offset:1024 sc1\n"
        "v_mfma_f32_16x16x32_bf16 v[72:75], v[140:143], a[176:179], v[72:75]\n"
        "v_mfma_f32_16x16x32_bf16 v[76:79], v[136:139], a[164:167], v[76:79]\n"
        "v_mfma_f32_16x16x32_bf16 v[76:79], v[140:143], a[180:183], v[76:79]\n"
        "v_mfma_f32_16x16x32_bf16 v[80:83], v[136:139], a[168:171], v[80:83]\n"
        "v_mfma_f32_16x16x32_bf16 v[80:83], v[140:143], a[184:187], v[80:83]\n"
        "v_mfma_f32_16x16x32_bf16 v[84:87], v[136:139], a[172:175], v[84:87]\n"
        "v_mfma_f32_16x16x32_bf16 v[84:87], v[140:143], a[188:191], v[84:87]\n"
        "s_waitcnt vmcnt(12)\n"
        "v_mfma_f32_16x16x32_bf16 v[72:75], v[144:147], a[192:195], v[72:75]\n"
        "global_load_dwordx4 v[136:139], v197, s[78:79] offset:0 sc1\n"
        "global_load_dwordx4 v[140:143], v197, s[78:79] offset:1024 sc1\n"
        "v_mfma_f32_16x16x32_bf16 v[72:75], v[148:151], a[208:211], v[72:75]\n"
        "v_mfma_f32_16x16x32_bf16 v[76:79], v[144:147], a[196:199], v[76:79]\n"
        "v_mfma_f32_16x16x32_bf16 v[76:79], v[148:151], a[212:215], v[76:79]\n"
        "v_mfma_f32_16x16x32_bf16 v[80:83], v[144:147], a[200:203], v[80:83]\n"
        "v_mfma_f32_16x16x32_bf16 v[80:83], v[148:151], a[216:219], v[80:83]\n"
        "v_mfma_f32_16x16x32_bf16 v[84:87], v[144:147], a[204:207], v[84:87]\n"
        "v_mfma_f32_16x16x32_bf16 v[84:87], v[148:151], a[220:223], v[84:87]\n"
        "s_waitcnt vmcnt(12)\n"
        "v_mfma_f32_16x16x32_bf16 v[72:75], v[152:155], a[224:227], v[72:75]\n"
        "global_load_dwordx4 v[144:147], v198, s[78:79] offset:0 sc1\n"
        "global_load_dwordx4 v[148:151], v198, s[78:79] offset:1024 sc1\n"
        "v_mfma_f32_16x16x32_bf16 v[72:75], v[156:159], a[240:243], v[72:75]\n"
        "v_mfma_f32_16x16x32_bf16 v[76:79], v[152:155], a[228:231], v[76:79]\n"
        "v_mfma_f32_16x16x32_bf16 v[76:79], v[156:159], a[244:247], v[76:79]\n"
        "v_mfma_f32_16x16x32_bf16 v[80:83], v[152:155], a[232:235], v[80:83]\n"
        "v_mfma_f32_16x16x32_bf16 v[80:83], v[156:159], a[248:251], v[80:83]\n"
        "v_mfma_f32_16x16x32_bf16 v[84:87], v[152:155], a[236:239], v[84:87]\n"
        "v_mfma_f32_16x16x32_bf16 v[84:87], v[156:159], a[252:255], v[84:87]\n"
        "s_waitcnt vmcnt(12)\n"
        "v_mfma_f32_16x16x32_bf16 v[56:59], v[96:99], a[0:3], 0\n"
        "global_load_dwordx4 v[152:155], v199, s[78:79] offset:0 sc1\n"
        "global_load_dwordx4 v[156:159], v199, s[78:79] offset:1024 sc1\n"
        "s_add_u32 s78, s78, 0x10080\n"
        "s_addc_u32 s79, s79, 0\n"
        "v_mfma_f32_16x16x32_bf16 v[56:59], v[100:103], a[16:19], v[56:59]\n"
        "v_mfma_f32_16x16x32_bf16 v[60:63], v[96:99], a[4:7], 0\n"
        "v_mfma_f32_16x16x32_bf16 v[60:63], v[100:103], a[20:23], v[60:63]\n"
        "v_mfma_f32_16x16x32_bf16 v[64:67], v[96:99], a[8:11], 0\n"
        "ds_write_b128 v47, v[76:79] offset:3072\n"
        "ds_write_b128 v47, v[80:83] offset:4096\n"
        "v_mfma_f32_16x16x32_bf16 v[64:67], v[100:103], a[24:27], v[64:67]\n"
        "ds_write_b128 v47, v[84:87] offset:5120\n"
        "v_mfma_f32_16x16x32_bf16 v[68:71], v[96:99], a[12:15], 0\n"
        "v_mfma_f32_16x16x32_bf16 v[68:71], v[100:103], a[28:31], v[68:71]\n"
        "s_waitcnt vmcnt(12)\n"
        "v_mfma_f32_16x16x32_bf16 v[56:59], v[104:107], a[32:35], v[56:59]\n"
        "global_load_dwordx4 v[96:99], v192, s[78:79] offset:0 sc1\n"
        "global_load_dwordx4 v[100:103], v192, s[78:79] offset:1024 sc1\n"
        "v_mfma_f32_16x16x32_bf16 v[56:59], v[108:111], a[48:51], v[56:59]\n"
        "v_mfma_f32_16x16x32_bf16 v[60:63], v[104:107], a[36:39], v[60:63]\n"
        "v_mfma_f32_16x16x32_bf16 v[60:63], v[108:111], a[52:55], v[60:63]\n"
        "v_mfma_f32_16x16x32_bf16 v[64:67], v[104:107], a[40:43], v[64:67]\n"
        "s_waitcnt lgkmcnt(0)\n"
        "s_barrier\n"
        "v_mfma_f32_16x16x32_bf16 v[64:67], v[108:111], a[56:59], v[64:67]\n"
        "ds_read_b128 v[4:7], v48 offset:3072\n"
        "ds_read_b128 v[8:11], v49 offset:3072\n"
        "v_mfma_f32_16x16x32_bf16 v[68:71], v[104:107], a[44:47], v[68:71]\n"
        "ds_read_b128 v[12:15], v50 offset:3072\n"
        "v_mfma_f32_16x16x32_bf16 v[68:71], v[108:111], a[60:63], v[68:71]\n"
        "s_waitcnt vmcnt(12)\n"
        "v_mfma_f32_16x16x32_bf16 v[56:59], v[112:115], a[64:67], v[56:59]\n"
        "global_load_dwordx4 v[104:107], v193, s[78:79] offset:0 sc1\n"
        "global_load_dwordx4 v[108:111], v193, s[78:79] offset:1024 sc1\n"
        "v_mfma_f32_16x16x32_bf16 v[56:59], v[116:119], a[80:83], v[56:59]\n"
        "v_mfma_f32_16x16x32_bf16 v[60:63], v[112:115], a[68:71], v[60:63]\n"
        "v_mfma_f32_16x16x32_bf16 v[60:63], v[116:119], a[84:87], v[60:63]\n"
        "v_mfma_f32_16x16x32_bf16 v[64:67], v[112:115], a[72:75], v[64:67]\n"
        "s_waitcnt lgkmcnt(0)\n"
        "v_add_f32 v4, v4, v8\n"
        "v_mfma_f32_16x16x32_bf16 v[64:67], v[116:119], a[88:91], v[64:67]\n"
        "v_add_f32 v5, v5, v9\n"
        "v_add_f32 v6, v6, v10\n"
        "v_mfma_f32_16x16x32_bf16 v[68:71], v[112:115], a[76:79], v[68:71]\n"
        "v_add_f32 v7, v7, v11\n"
        "v_add_f32 v12, v72, v12\n"
        "v_mfma_f32_16x16x32_bf16 v[68:71], v[116:119], a[92:95], v[68:71]\n"
        "v_add_f32 v13, v73, v13\n"
        "s_waitcnt vmcnt(12)\n"
        "v_mfma_f32_16x16x32_bf16 v[56:59], v[120:123], a[96:99], v[56:59]\n"
        "global_load_dwordx4 v[112:115], v194, s[78:79] offset:0 sc1\n"
        "global_load_dwordx4 v[116:119], v194, s[78:79] offset:1024 sc1\n"
        "v_mfma_f32_16x16x32_bf16 v[56:59], v[124:127], a[112:115], v[56:59]\n"
        "v_add_f32 v14, v74, v14\n"
        "v_add_f32 v15, v75, v15\n"
        "v_mfma_f32_16x16x32_bf16 v[60:63], v[120:123], a[100:103], v[60:63]\n"
        "v_add_f32 v12, v12, v4\n"
        "v_add_f32 v13, v13, v5\n"
        "v_mfma_f32_16x16x32_bf16 v[60:63], v[124:127], a[116:119], v[60:63]\n"
        "v_add_f32 v14, v14, v6\n"
        "v_add_f32 v15, v15, v7\n"
        "v_mfma_f32_16x16x32_bf16 v[64:67], v[120:123], a[104:107], v[64:67]\n"
        "v_cvt_pk_bf16_f32 v234, v12, v13\n"
        "v_cvt_pk_bf16_f32 v235, v14, v15\n"
        "v_mfma_f32_16x16x32_bf16 v[64:67], v[124:127], a[120:123], v[64:67]\n"
        "v_min_u32 v234, -2, v234\n"
        "v_min_u32 v235, -2, v235\n"
        "v_mfma_f32_16x16x32_bf16 v[68:71], v[120:123], a[108:111], v[68:71]\n"
        "v_mfma_f32_16x16x32_bf16 v[68:71], v[124:127], a[124:127], v[68:71]\n"
        "s_waitcnt vmcnt(12)\n"
        "v_mfma_f32_16x16x32_bf16 v[56:59], v[128:131], a[128:131], v[56:59]\n"
        "global_load_dwordx4 v[120:123], v195, s[78:79] offset:0 sc1\n"
        "global_load_dwordx4 v[124:127], v195, s[78:79] offset:1024 sc1\n"
        "v_mfma_f32_16x16x32_bf16 v[56:59], v[132:135], a[144:147], v[56:59]\n"
        "global_store_dwordx4 v44, v[228:231], s[40:41] offset:0 sc1\n"
        "global_store_dwordx4 v44, v[232:235], s[40:41] offset:1024 sc1\n"
        "v_mfma_f32_16x16x32_bf16 v[60:63], v[128:131], a[132:135], v[60:63]\n"
        "v_mfma_f32_16x16x32_bf16 v[60:63], v[132:135], a[148:151], v[60:63]\n"
        "v_mfma_f32_16x16x32_bf16 v[64:67], v[128:131], a[136:139], v[64:67]\n"
        "v_mfma_f32_16x16x32_bf16 v[64:67], v[132:135], a[152:155], v[64:67]\n"
        "v_mfma_f32_16x16x32_bf16 v[68:71], v[128:131], a[140:143], v[68:71]\n"
        "v_mfma_f32_16x16x32_bf16 v[68:71], v[132:135], a[156:159], v[68:71]\n"
        "s_waitcnt vmcnt(14)\n"
        "v_mfma_f32_16x16x32_bf16 v[56:59], v[136:139], a[160:163], v[56:59]\n"
        "global_load_dwordx4 v[128:131], v196, s[78:79] offset:0 sc1\n"
        "global_load_dwordx4 v[132:135], v196, s[78:79] offset:1024 sc1\n"
        "v_mfma_f32_16x16x32_bf16 v[56:59], v[140:143], a[176:179], v[56:59]\n"
        "v_mfma_f32_16x16x32_bf16 v[60:63], v[136:139], a[164:167], v[60:63]\n"
        "v_mfma_f32_16x16x32_bf16 v[60:63], v[140:143], a[180:183], v[60:63]\n"
        "v_mfma_f32_16x16x32_bf16 v[64:67], v[136:139], a[168:171], v[64:67]\n"
        "v_mfma_f32_16x16x32_bf16 v[64:67], v[140:143], a[184:187], v[64:67]\n"
        "v_mfma_f32_16x16x32_bf16 v[68:71], v[136:139], a[172:175], v[68:71]\n"
        "v_mfma_f32_16x16x32_bf16 v[68:71], v[140:143], a[188:191], v[68:71]\n"
        "s_waitcnt vmcnt(14)\n"
        "v_mfma_f32_16x16x32_bf16 v[56:59], v[144:147], a[192:195], v[56:59]\n"
        "global_load_dwordx4 v[136:139], v197, s[78:79] offset:0 sc1\n"
        "global_load_dwordx4 v[140:143], v197, s[78:79] offset:1024 sc1\n"
        "v_mfma_f32_16x16x32_bf16 v[56:59], v[148:151], a[208:211], v[56:59]\n"
        "v_mfma_f32_16x16x32_bf16 v[60:63], v[144:147], a[196:199], v[60:63]\n"
        "v_mfma_f32_16x16x32_bf16 v[60:63], v[148:151], a[212:215], v[60:63]\n"
        "v_mfma_f32_16x16x32_bf16 v[64:67], v[144:147], a[200:203], v[64:67]\n"
        "v_mfma_f32_16x16x32_bf16 v[64:67], v[148:151], a[216:219], v[64:67]\n"
        "v_mfma_f32_16x16x32_bf16 v[68:71], v[144:147], a[204:207], v[68:71]\n"
        "v_mfma_f32_16x16x32_bf16 v[68:71], v[148:151], a[220:223], v[68:71]\n"
        "s_waitcnt vmcnt(14)\n"
        "v_mfma_f32_16x16x32_bf16 v[56:59], v[152:155], a[224:227], v[56:59]\n"
        "global_load_dwordx4 v[144:147], v198, s[78:79] offset:0 sc1\n"
        "global_load_dwordx4 v[148:151], v198, s[78:79] offset:1024 sc1\n"
        "v_mfma_f32_16x16x32_bf16 v[56:59], v[156:159], a[240:243], v[56:59]\n"
        "v_mfma_f32_16x16x32_bf16 v[60:63], v[152:155], a[228:231], v[60:63]\n"
        "v_mfma_f32_16x16x32_bf16 v[60:63], v[156:159], a[244:247], v[60:63]\n"
        "v_mfma_f32_16x16x32_bf16 v[64:67], v[152:155], a[232:235], v[64:67]\n"
        "v_mfma_f32_16x16x32_bf16 v[64:67], v[156:159], a[248:251], v[64:67]\n"
        "v_mfma_f32_16x16x32_bf16 v[68:71], v[152:155], a[236:239], v[68:71]\n"
        "v_mfma_f32_16x16x32_bf16 v[68:71], v[156:159], a[252:255], v[68:71]\n"
        "s_waitcnt vmcnt(14)\n"
        "v_mfma_f32_16x16x32_bf16 v[72:75], v[96:99], a[0:3], 0\n"
        "global_load_dwordx4 v[152:155], v199, s[78:79] offset:0 sc1\n"
        "global_load_dwordx4 v[156:159], v199, s[78:79] offset:1024 sc1\n"
        "s_add_u32 s78, s78, 0x10080\n"
        "s_addc_u32 s79, s79, 0\n"
        "v_mfma_f32_16x16x32_bf16 v[72:75], v[100:103], a[16:19], v[72:75]\n"
        "v_mfma_f32_16x16x32_bf16 v[76:79], v[96:99], a[4:7], 0\n"
        "v_mfma_f32_16x16x32_bf16 v[76:79], v[100:103], a[20:23], v[76:79]\n"
        "v_mfma_f32_16x16x32_bf16 v[80:83], v[96:99], a[8:11], 0\n"
        "ds_write_b128 v47, v[60:63] offset:0\n"
        "ds_write_b128 v47, v[64:67] offset:1024\n"
        "v_mfma_f32_16x16x32_bf16 v[80:83], v[100:103], a[24:27], v[80:83]\n"
        "ds_write_b128 v47, v[68:71] offset:2048\n"
        "v_mfma_f32_16x16x32_bf16 v[84:87], v[96:99], a[12:15], 0\n"
        "v_mfma_f32_16x16x32_bf16 v[84:87], v[100:103], a[28:31], v[84:87]\n"
        "s_waitcnt vmcnt(14)\n"
        "v_mfma_f32_16x16x32_bf16 v[72:75], v[104:107], a[32:35], v[72:75]\n"
        "global_load_dwordx4 v[96:99], v192, s[78:79] offset:0 sc1\n"
        "global_load_dwordx4 v[100:103], v192, s[78:79] offset:1024 sc1\n"
        "v_mfma_f32_16x16x32_bf16 v[72:75], v[108:111], a[48:51], v[72:75]\n"
        "v_mfma_f32_16x16x32_bf16 v[76:79], v[104:107], a[36:39], v[76:79]\n"
        "v_mfma_f32_16x16x32_bf16 v[76:79], v[108:111], a[52:55], v[76:79]\n"
        "v_mfma_f32_16x16x32_bf16 v[80:83], v[104:107], a[40:43], v[80:83]\n"
        "s_waitcnt lgkmcnt(0)\n"
        "s_barrier\n"
        "v_mfma_f32_16x16x32_bf16 v[80:83], v[108:111], a[56:59], v[80:83]\n"
        "ds_read_b128 v[4:7], v48 offset:0\n"
        "ds_read_b128 v[8:11], v49 offset:0\n"
        "v_mfma_f32_16x16x32_bf16 v[84:87], v[104:107], a[44:47], v[84:87]\n"
        "ds_read_b128 v[12:15], v50 offset:0\n"
        "v_mfma_f32_16x16x32_bf16 v[84:87], v[108:111], a[60:63], v[84:87]\n"
        "s_waitcnt vmcnt(14)\n"
        "v_mfma_f32_16x16x32_bf16 v[72:75], v[112:115], a[64:67], v[72:75]\n"
        "global_load_dwordx4 v[104:107], v193, s[78:79] offset:0 sc1\n"
        "global_load_dwordx4 v[108:111], v193, s[78:79] offset:1024 sc1\n"
        "v_mfma_f32_16x16x32_bf16 v[72:75], v[116:119], a[80:83], v[72:75]\n"
        "v_mfma_f32_16x16x32_bf16 v[76:79], v[112:115], a[68:71], v[76:79]\n"
        "v_mfma_f32_16x16x32_bf16 v[76:79], v[116:119], a[84:87], v[76:79]\n"
        "v_mfma_f32_16x16x32_bf16 v[80:83], v[112:115], a[72:75], v[80:83]\n"
        "s_waitcnt lgkmcnt(0)\n"
        "v_add_f32 v4, v4, v8\n"
        "v_mfma_f32_16x16x32_bf16 v[80:83], v[116:119], a[88:91], v[80:83]\n"
        "v_add_f32 v5, v5, v9\n"
        "v_add_f32 v6, v6, v10\n"
        "v_mfma_f32_16x16x32_bf16 v[84:87], v[112:115], a[76:79], v[84:87]\n"
        "v_add_f32 v7, v7, v11\n"
        "v_add_f32 v208, v56, v12\n"
        "v_mfma_f32_16x16x32_bf16 v[84:87], v[116:119], a[92:95], v[84:87]\n"
        "v_add_f32 v209, v57, v13\n"
        "s_waitcnt vmcnt(14)\n"
        "v_mfma_f32_16x16x32_bf16 v[72:75], v[120:123], a[96:99], v[72:75]\n"
        "global_load_dwordx4 v[112:115], v194, s[78:79] offset:0 sc1\n"
        "global_load_dwordx4 v[116:119], v194, s[78:79] offset:1024 sc1\n"
        "v_mfma_f32_16x16x32_bf16 v[72:75], v[124:127], a[112:115], v[72:75]\n"
        "v_add_f32 v210, v58, v14\n"
        "v_add_f32 v211, v59, v15\n"
        "v_mfma_f32_16x16x32_bf16 v[76:79], v[120:123], a[100:103], v[76:79]\n"
        "v_add_f32 v208, v208, v4\n"
        "v_add_f32 v209, v209, v5\n"
        "v_mfma_f32_16x16x32_bf16 v[76:79], v[124:127], a[116:119], v[76:79]\n"
        "v_add_f32 v210, v210, v6\n"
        "v_add_f32 v211, v211, v7\n"
        "v_mfma_f32_16x16x32_bf16 v[80:83], v[120:123], a[104:107], v[80:83]\n"
        "v_mfma_f32_16x16x32_bf16 v[80:83], v[124:127], a[120:123], v[80:83]\n"
        "v_mfma_f32_16x16x32_bf16 v[84:87], v[120:123], a[108:111], v[84:87]\n"
        "v_mfma_f32_16x16x32_bf16 v[84:87], v[124:127], a[124:127], v[84:87]\n"
        "s_waitcnt vmcnt(12)\n"
        "v_mfma_f32_16x16x32_bf16 v[72:75], v[128:131], a[128:131], v[72:75]\n"
        "global_load_dwordx4 v[120:123], v195, s[78:79] offset:0 sc1\n"
        "global_load_dwordx4 v[124:127], v195, s[78:79] offset:1024 sc1\n"
        "v_mfma_f32_16x16x32_bf16 v[72:75], v[132:135], a[144:147], v[72:75]\n"
        "v_mfma_f32_16x16x32_bf16 v[76:79], v[128:131], a[132:135], v[76:79]\n"
        "v_mfma_f32_16x16x32_bf16 v[76:79], v[132:135], a[148:151], v[76:79]\n"
        "v_mfma_f32_16x16x32_bf16 v[80:83], v[128:131], a[136:139], v[80:83]\n"
        "v_mfma_f32_16x16x32_bf16 v[80:83], v[132:135], a[152:155], v[80:83]\n"
        "v_mfma_f32_16x16x32_bf16 v[84:87], v[128:131], a[140:143], v[84:87]\n"
        "v_mfma_f32_16x16x32_bf16 v[84:87], v[132:135], a[156:159], v[84:87]\n"
        "s_waitcnt vmcnt(12)\n"
        "v_mfma_f32_16x16x32_bf16 v[72:75], v[136:139], a[160:163], v[72:75]\n"
        "global_load_dwordx4 v[128:131], v196, s[78:79] offset:0 sc1\n"
        "global_load_dwordx4 v[132:135], v196, s[78:79] offset:1024 sc1\n"
        "v_mfma_f32_16x16x32_bf16 v[72:75], v[140:143], a[176:179], v[72:75]\n"
        "v_mfma_f32_16x16x32_bf16 v[76:79], v[136:139], a[164:167], v[76:79]\n"
        "v_mfma_f32_16x16x32_bf16 v[76:79], v[140:143], a[180:183], v[76:79]\n"
        "v_mfma_f32_16x16x32_bf16 v[80:83], v[136:139], a[168:171], v[80:83]\n"
        "v_mfma_f32_16x16x32_bf16 v[80:83], v[140:143], a[184:187], v[80:83]\n"
        "v_mfma_f32_16x16x32_bf16 v[84:87], v[136:139], a[172:175], v[84:87]\n"
        "v_mfma_f32_16x16x32_bf16 v[84:87], v[140:143], a[188:191], v[84:87]\n"
        "s_waitcnt vmcnt(12)\n"
        "v_mfma_f32_16x16x32_bf16 v[72:75], v[144:147], a[192:195], v[72:75]\n"
        "global_load_dwordx4 v[136:139], v197, s[78:79] offset:0 sc1\n"
        "global_load_dwordx4 v[140:143], v197, s[78:79] offset:1024 sc1\n"
        "v_mfma_f32_16x16x32_bf16 v[72:75], v[148:151], a[208:211], v[72:75]\n"
        "v_mfma_f32_16x16x32_bf16 v[76:79], v[144:147], a[196:199], v[76:79]\n"
        "v_mfma_f32_16x16x32_bf16 v[76:79], v[148:151], a[212:215], v[76:79]\n"
        "v_mfma_f32_16x16x32_bf16 v[80:83], v[144:147], a[200:203], v[80:83]\n"
        "v_mfma_f32_16x16x32_bf16 v[80:83], v[148:151], a[216:219], v[80:83]\n"
        "v_mfma_f32_16x16x32_bf16 v[84:87], v[144:147], a[204:207], v[84:87]\n"
        "v_mfma_f32_16x16x32_bf16 v[84:87], v[148:151], a[220:223], v[84:87]\n"
        "s_waitcnt vmcnt(12)\n"
        "v_mfma_f32_16x16x32_bf16 v[72:75], v[152:155], a[224:227], v[72:75]\n"
        "global_load_dwordx4 v[144:147], v198, s[78:79] offset:0 sc1\n"
        "global_load_dwordx4 v[148:151], v198, s[78:79] offset:1024 sc1\n"
        "v_mfma_f32_16x16x32_bf16 v[72:75], v[156:159], a[240:243], v[72:75]\n"
        "v_mfma_f32_16x16x32_bf16 v[76:79], v[152:155], a[228:231], v[76:79]\n"
        "v_mfma_f32_16x16x32_bf16 v[76:79], v[156:159], a[244:247], v[76:79]\n"
        "v_mfma_f32_16x16x32_bf16 v[80:83], v[152:155], a[232:235], v[80:83]\n"
        "v_mfma_f32_16x16x32_bf16 v[80:83], v[156:159], a[248:251], v[80:83]\n"
        "v_mfma_f32_16x16x32_bf16 v[84:87], v[152:155], a[236:239], v[84:87]\n"
        "v_mfma_f32_16x16x32_bf16 v[84:87], v[156:159], a[252:255], v[84:87]\n"
        "s_waitcnt vmcnt(12)\n"
        "v_mfma_f32_16x16x32_bf16 v[56:59], v[96:99], a[0:3], 0\n"
        "global_load_dwordx4 v[152:155], v199, s[78:79] offset:0 sc1\n"
        "global_load_dwordx4 v[156:159], v199, s[78:79] offset:1024 sc1\n"
        "s_add_u32 s78, s78, 0x10080\n"
        "s_addc_u32 s79, s79, 0\n"
        "v_mfma_f32_16x16x32_bf16 v[56:59], v[100:103], a[16:19], v[56:59]\n"
        "v_mfma_f32_16x16x32_bf16 v[60:63], v[96:99], a[4:7], 0\n"
        "v_mfma_f32_16x16x32_bf16 v[60:63], v[100:103], a[20:23], v[60:63]\n"
        "v_mfma_f32_16x16x32_bf16 v[64:67], v[96:99], a[8:11], 0\n"
        "ds_write_b128 v47, v[76:79] offset:3072\n"
        "ds_write_b128 v47, v[80:83] offset:4096\n"
        "v_mfma_f32_16x16x32_bf16 v[64:67], v[100:103], a[24:27], v[64:67]\n"
        "ds_write_b128 v47, v[84:87] offset:5120\n"
        "v_mfma_f32_16x16x32_bf16 v[68:71], v[96:99], a[12:15], 0\n"
        "v_mfma_f32_16x16x32_bf16 v[68:71], v[100:103], a[28:31], v[68:71]\n"
        "s_waitcnt vmcnt(12)\n"
        "v_mfma_f32_16x16x32_bf16 v[56:59], v[104:107], a[32:35], v[56:59]\n"
        "global_load_dwordx4 v[96:99], v192, s[78:79] offset:0 sc1\n"
        "global_load_dwordx4 v[100:103], v192, s[78:79] offset:1024 sc1\n"
        "v_mfma_f32_16x16x32_bf16 v[56:59], v[108:111], a[48:51], v[56:59]\n"
        "v_mfma_f32_16x16x32_bf16 v[60:63], v[104:107], a[36:39], v[60:63]\n"
        "v_mfma_f32_16x16x32_bf16 v[60:63], v[108:111], a[52:55], v[60:63]\n"
        "v_mfma_f32_16x16x32_bf16 v[64:67], v[104:107], a[40:43], v[64:67]\n"
        "s_waitcnt lgkmcnt(0)\n"
        "s_barrier\n"
        "v_mfma_f32_16x16x32_bf16 v[64:67], v[108:111], a[56:59], v[64:67]\n"
        "ds_read_b128 v[4:7], v48 offset:3072\n"
        "ds_read_b128 v[8:11], v49 offset:3072\n"
        "v_mfma_f32_16x16x32_bf16 v[68:71], v[104:107], a[44:47], v[68:71]\n"
        "ds_read_b128 v[12:15], v50 offset:3072\n"
        "v_mfma_f32_16x16x32_bf16 v[68:71], v[108:111], a[60:63], v[68:71]\n"
        "s_waitcnt vmcnt(12)\n"
        "v_mfma_f32_16x16x32_bf16 v[56:59], v[112:115], a[64:67], v[56:59]\n"
        "global_load_dwordx4 v[104:107], v193, s[78:79] offset:0 sc1\n"
        "global_load_dwordx4 v[108:111], v193, s[78:79] offset:1024 sc1\n"
        "v_mfma_f32_16x16x32_bf16 v[56:59], v[116:119], a[80:83], v[56:59]\n"
        "v_mfma_f32_16x16x32_bf16 v[60:63], v[112:115], a[68:71], v[60:63]\n"
        "v_mfma_f32_16x16x32_bf16 v[60:63], v[116:119], a[84:87], v[60:63]\n"
        "v_mfma_f32_16x16x32_bf16 v[64:67], v[112:115], a[72:75], v[64:67]\n"
        "s_waitcnt lgkmcnt(0)\n"
        "v_add_f32 v4, v4, v8\n"
        "v_mfma_f32_16x16x32_bf16 v[64:67], v[116:119], a[88:91], v[64:67]\n"
        "v_add_f32 v5, v5, v9\n"
        "v_add_f32 v6, v6, v10\n"
        "v_mfma_f32_16x16x32_bf16 v[68:71], v[112:115], a[76:79], v[68:71]\n"
        "v_add_f32 v7, v7, v11\n"
        "v_add_f32 v212, v72, v12\n"
        "v_mfma_f32_16x16x32_bf16 v[68:71], v[116:119], a[92:95], v[68:71]\n"
        "v_add_f32 v213, v73, v13\n"
        "s_waitcnt vmcnt(12)\n"
        "v_mfma_f32_16x16x32_bf16 v[56:59], v[120:123], a[96:99], v[56:59]\n"
        "global_load_dwordx4 v[112:115], v194, s[78:79] offset:0 sc1\n"
        "global_load_dwordx4 v[116:119], v194, s[78:79] offset:1024 sc1\n"
        "v_mfma_f32_16x16x32_bf16 v[56:59], v[124:127], a[112:115], v[56:59]\n"
        "v_add_f32 v214, v74, v14\n"
        "v_add_f32 v215, v75, v15\n"
        "v_mfma_f32_16x16x32_bf16 v[60:63], v[120:123], a[100:103], v[60:63]\n"
        "v_add_f32 v212, v212, v4\n"
        "v_add_f32 v213, v213, v5\n"
        "v_mfma_f32_16x16x32_bf16 v[60:63], v[124:127], a[116:119], v[60:63]\n"
        "v_add_f32 v214, v214, v6\n"
        "v_add_f32 v215, v215, v7\n"
        "v_mfma_f32_16x16x32_bf16 v[64:67], v[120:123], a[104:107], v[64:67]\n"
        "v_mfma_f32_16x16x32_bf16 v[64:67], v[124:127], a[120:123], v[64:67]\n"
        "v_mfma_f32_16x16x32_bf16 v[68:71], v[120:123], a[108:111], v[68:71]\n"
        "v_mfma_f32_16x16x32_bf16 v[68:71], v[124:127], a[124:127], v[68:71]\n"
        "s_waitcnt vmcnt(12)\n"
        "v_mfma_f32_16x16x32_bf16 v[56:59], v[128:131], a[128:131], v[56:59]\n"
        "global_load_dwordx4 v[120:123], v195, s[78:79] offset:0 sc1\n"
        "global_load_dwordx4 v[124:127], v195, s[78:79] offset:1024 sc1\n"
        "v_mfma_f32_16x16x32_bf16 v[56:59], v[132:135], a[144:147], v[56:59]\n"
        "v_mfma_f32_16x16x32_bf16 v[60:63], v[128:131], a[132:135], v[60:63]\n"
        "v_mfma_f32_16x16x32_bf16 v[60:63], v[132:135], a[148:151], v[60:63]\n"
        "v_mfma_f32_16x16x32_bf16 v[64:67], v[128:131], a[136:139], v[64:67]\n"
        "v_mfma_f32_16x16x32_bf16 v[64:67], v[132:135], a[152:155], v[64:67]\n"
        "v_mfma_f32_16x16x32_bf16 v[68:71], v[128:131], a[140:143], v[68:71]\n"
        "v_mfma_f32_16x16x32_bf16 v[68:71], v[132:135], a[156:159], v[68:71]\n"
        "s_waitcnt vmcnt(12)\n"
        "v_mfma_f32_16x16x32_bf16 v[56:59], v[136:139], a[160:163], v[56:59]\n"
        "global_load_dwordx4 v[128:131], v196, s[78:79] offset:0 sc1\n"
        "global_load_dwordx4 v[132:135], v196, s[78:79] offset:1024 sc1\n"
        "v_mfma_f32_16x16x32_bf16 v[56:59], v[140:143], a[176:179], v[56:59]\n"
        "v_mfma_f32_16x16x32_bf16 v[60:63], v[136:139], a[164:167], v[60:63]\n"
        "v_mfma_f32_16x16x32_bf16 v[60:63], v[140:143], a[180:183], v[60:63]\n"
        "v_mfma_f32_16x16x32_bf16 v[64:67], v[136:139], a[168:171], v[64:67]\n"
        "v_mfma_f32_16x16x32_bf16 v[64:67], v[140:143], a[184:187], v[64:67]\n"
        "v_mfma_f32_16x16x32_bf16 v[68:71], v[136:139], a[172:175], v[68:71]\n"
        "v_mfma_f32_16x16x32_bf16 v[68:71], v[140:143], a[188:191], v[68:71]\n"
        "s_waitcnt vmcnt(12)\n"
        "v_mfma_f32_16x16x32_bf16 v[56:59], v[144:147], a[192:195], v[56:59]\n"
        "global_load_dwordx4 v[136:139], v197, s[78:79] offset:0 sc1\n"
        "global_load_dwordx4 v[140:143], v197, s[78:79] offset:1024 sc1\n"
        "v_mfma_f32_16x16x32_bf16 v[56:59], v[148:151], a[208:211], v[56:59]\n"
        "v_mfma_f32_16x16x32_bf16 v[60:63], v[144:147], a[196:199], v[60:63]\n"
        "v_mfma_f32_16x16x32_bf16 v[60:63], v[148:151], a[212:215], v[60:63]\n"
        "v_mfma_f32_16x16x32_bf16 v[64:67], v[144:147], a[200:203], v[64:67]\n"
        "v_mfma_f32_16x16x32_bf16 v[64:67], v[148:151], a[216:219], v[64:67]\n"
        "v_mfma_f32_16x16x32_bf16 v[68:71], v[144:147], a[204:207], v[68:71]\n"
        "v_mfma_f32_16x16x32_bf16 v[68:71], v[148:151], a[220:223], v[68:71]\n"
        "s_waitcnt vmcnt(12)\n"
        "v_mfma_f32_16x16x32_bf16 v[56:59], v[152:155], a[224:227], v[56:59]\n"
        "global_load_dwordx4 v[144:147], v198, s[78:79] offset:0 sc1\n"
        "global_load_dwordx4 v[148:151], v198, s[78:79] offset:1024 sc1\n"
        "v_mfma_f32_16x16x32_bf16 v[56:59], v[156:159], a[240:243], v[56:59]\n"
        "v_mfma_f32_16x16x32_bf16 v[60:63], v[152:155], a[228:231], v[60:63]\n"
        "v_mfma_f32_16x16x32_bf16 v[60:63], v[156:159], a[244:247], v[60:63]\n"
        "v_mfma_f32_16x16x32_bf16 v[64:67], v[152:155], a[232:235], v[64:67]\n"
        "v_mfma_f32_16x16x32_bf16 v[64:67], v[156:159], a[248:251], v[64:67]\n"
        "v_mfma_f32_16x16x32_bf16 v[68:71], v[152:155], a[236:239], v[68:71]\n"
        "v_mfma_f32_16x16x32_bf16 v[68:71], v[156:159], a[252:255], v[68:71]\n"
        "s_waitcnt vmcnt(12)\n"
        "v_mfma_f32_16x16x32_bf16 v[72:75], v[96:99], a[0:3], 0\n"
        "global_load_dwordx4 v[152:155], v199, s[78:79] offset:0 sc1\n"
        "global_load_dwordx4 v[156:159], v199, s[78:79] offset:1024 sc1\n"
        "s_add_u32 s78, s78, 0x10080\n"
        "s_addc_u32 s79, s79, 0\n"
        "v_mfma_f32_16x16x32_bf16 v[72:75], v[100:103], a[16:19], v[72:75]\n"
        "global_load_dwordx4 v[240:243], v44, s[38:39] offset:0 sc1\n"
        "global_load_dwordx4 v[244:247], v44, s[38:39] offset:1024 sc1\n"
        "v_mfma_f32_16x16x32_bf16 v[76:79], v[96:99], a[4:7], 0\n"
        "global_load_dwordx2 v[248:249], v44, s[38:39] offset:2048 sc1\n"
        "v_mfma_f32_16x16x32_bf16 v[76:79], v[100:103], a[20:23], v[76:79]\n"
        "v_mfma_f32_16x16x32_bf16 v[80:83], v[96:99], a[8:11], 0\n"
        "ds_write_b128 v47, v[60:63] offset:0\n"
        "ds_write_b128 v47, v[64:67] offset:1024\n"
        "v_mfma_f32_16x16x32_bf16 v[80:83], v[100:103], a[24:27], v[80:83]\n"
        "ds_write_b128 v47, v[68:71] offset:2048\n"
        "v_mfma_f32_16x16x32_bf16 v[84:87], v[96:99], a[12:15], 0\n"
        "v_mfma_f32_16x16x32_bf16 v[84:87], v[100:103], a[28:31], v[84:87]\n"
        "s_waitcnt vmcnt(15)\n"
        "v_mfma_f32_16x16x32_bf16 v[72:75], v[104:107], a[32:35], v[72:75]\n"
        "global_load_dwordx4 v[96:99], v192, s[78:79] offset:0 sc1\n"
        "global_load_dwordx4 v[100:103], v192, s[78:79] offset:1024 sc1\n"
        "v_mfma_f32_16x16x32_bf16 v[72:75], v[108:111], a[48:51], v[72:75]\n"
        "v_mfma_f32_16x16x32_bf16 v[76:79], v[104:107], a[36:39], v[76:79]\n"
        "v_mfma_f32_16x16x32_bf16 v[76:79], v[108:111], a[52:55], v[76:79]\n"
        "v_mfma_f32_16x16x32_bf16 v[80:83], v[104:107], a[40:43], v[80:83]\n"
        "s_waitcnt lgkmcnt(0)\n"
        "s_barrier\n"
        "v_mfma_f32_16x16x32_bf16 v[80:83], v[108:111], a[56:59], v[80:83]\n"
        "ds_read_b128 v[4:7], v48 offset:0\n"
        "ds_read_b128 v[8:11], v49 offset:0\n"
        "v_mfma_f32_16x16x32_bf16 v[84:87], v[104:107], a[44:47], v[84:87]\n"
        "ds_read_b128 v[12:15], v50 offset:0\n"
        "v_mfma_f32_16x16x32_bf16 v[84:87], v[108:111], a[60:63], v[84:87]\n"
        "s_waitcnt vmcnt(15)\n"
        "v_mfma_f32_16x16x32_bf16 v[72:75], v[112:115], a[64:67], v[72:75]\n"
        "global_load_dwordx4 v[104:107], v193, s[78:79] offset:0 sc1\n"
        "global_load_dwordx4 v[108:111], v193, s[78:79] offset:1024 sc1\n"
        "v_mfma_f32_16x16x32_bf16 v[72:75], v[116:119], a[80:83], v[72:75]\n"
        "v_mfma_f32_16x16x32_bf16 v[76:79], v[112:115], a[68:71], v[76:79]\n"
        "v_mfma_f32_16x16x32_bf16 v[76:79], v[116:119], a[84:87], v[76:79]\n"
        "v_mfma_f32_16x16x32_bf16 v[80:83], v[112:115], a[72:75], v[80:83]\n"
        "s_waitcnt lgkmcnt(0)\n"
        "v_add_f32 v4, v4, v8\n"
        "v_mfma_f32_16x16x32_bf16 v[80:83], v[116:119], a[88:91], v[80:83]\n"
        "v_add_f32 v5, v5, v9\n"
        "v_add_f32 v6, v6, v10\n"
        "v_mfma_f32_16x16x32_bf16 v[84:87], v[112:115], a[76:79], v[84:87]\n"
        "v_add_f32 v7, v7, v11\n"
        "v_add_f32 v216, v56, v12\n"
        "v_mfma_f32_16x16x32_bf16 v[84:87], v[116:119], a[92:95], v[84:87]\n"
        "v_add_f32 v217, v57, v13\n"
        "s_waitcnt vmcnt(15)\n"
        "v_mfma_f32_16x16x32_bf16 v[72:75], v[120:123], a[96:99], v[72:75]\n"
        "global_load_dwordx4 v[112:115], v194, s[78:79] offset:0 sc1\n"
        "global_load_dwordx4 v[116:119], v194, s[78:79] offset:1024 sc1\n"
        "v_mfma_f32_16x16x32_bf16 v[72:75], v[124:127], a[112:115], v[72:75]\n"
        "v_add_f32 v218, v58, v14\n"
        "v_add_f32 v219, v59, v15\n"
        "v_mfma_f32_16x16x32_bf16 v[76:79], v[120:123], a[100:103], v[76:79]\n"
        "v_add_f32 v216, v216, v4\n"
        "v_add_f32 v217, v217, v5\n"
        "v_mfma_f32_16x16x32_bf16 v[76:79], v[124:127], a[116:119], v[76:79]\n"
        "v_add_f32 v218, v218, v6\n"
        "v_add_f32 v219, v219, v7\n"
        "v_mfma_f32_16x16x32_bf16 v[80:83], v[120:123], a[104:107], v[80:83]\n"
        "v_mfma_f32_16x16x32_bf16 v[80:83], v[124:127], a[120:123], v[80:83]\n"
        "v_mfma_f32_16x16x32_bf16 v[84:87], v[120:123], a[108:111], v[84:87]\n"
        "v_mfma_f32_16x16x32_bf16 v[84:87], v[124:127], a[124:127], v[84:87]\n"
        "s_waitcnt vmcnt(15)\n"
        "v_mfma_f32_16x16x32_bf16 v[72:75], v[128:131], a[128:131], v[72:75]\n"
        "global_load_dwordx4 v[120:123], v195, s[78:79] offset:0 sc1\n"
        "global_load_dwordx4 v[124:127], v195, s[78:79] offset:1024 sc1\n"
        "v_mfma_f32_16x16x32_bf16 v[72:75], v[132:135], a[144:147], v[72:75]\n"
        "v_mfma_f32_16x16x32_bf16 v[76:79], v[128:131], a[132:135], v[76:79]\n"
        "v_mfma_f32_16x16x32_bf16 v[76:79], v[132:135], a[148:151], v[76:79]\n"
        "v_mfma_f32_16x16x32_bf16 v[80:83], v[128:131], a[136:139], v[80:83]\n"
        "v_mfma_f32_16x16x32_bf16 v[80:83], v[132:135], a[152:155], v[80:83]\n"
        "v_mfma_f32_16x16x32_bf16 v[84:87], v[128:131], a[140:143], v[84:87]\n"
        "v_mfma_f32_16x16x32_bf16 v[84:87], v[132:135], a[156:159], v[84:87]\n"
        "s_waitcnt vmcnt(15)\n"
        "v_mfma_f32_16x16x32_bf16 v[72:75], v[136:139], a[160:163], v[72:75]\n"
        "global_load_dwordx4 v[128:131], v196, s[78:79] offset:0 sc1\n"
        "global_load_dwordx4 v[132:135], v196, s[78:79] offset:1024 sc1\n"
        "v_mfma_f32_16x16x32_bf16 v[72:75], v[140:143], a[176:179], v[72:75]\n"
        "v_mfma_f32_16x16x32_bf16 v[76:79], v[136:139], a[164:167], v[76:79]\n"
        "v_mfma_f32_16x16x32_bf16 v[76:79], v[140:143], a[180:183], v[76:79]\n"
        "v_mfma_f32_16x16x32_bf16 v[80:83], v[136:139], a[168:171], v[80:83]\n"
        "v_mfma_f32_16x16x32_bf16 v[80:83], v[140:143], a[184:187], v[80:83]\n"
        "v_mfma_f32_16x16x32_bf16 v[84:87], v[136:139], a[172:175], v[84:87]\n"
        "v_mfma_f32_16x16x32_bf16 v[84:87], v[140:143], a[188:191], v[84:87]\n"
        "s_waitcnt vmcnt(15)\n"
        "v_mfma_f32_16x16x32_bf16 v[72:75], v[144:147], a[192:195], v[72:75]\n"
        "global_load_dwordx4 v[136:139], v197, s[78:79] offset:0 sc1\n"
        "global_load_dwordx4 v[140:143], v197, s[78:79] offset:1024 sc1\n"
        "v_mfma_f32_16x16x32_bf16 v[72:75], v[148:151], a[208:211], v[72:75]\n"
        "v_mfma_f32_16x16x32_bf16 v[76:79], v[144:147], a[196:199], v[76:79]\n"
        "v_mfma_f32_16x16x32_bf16 v[76:79], v[148:151], a[212:215], v[76:79]\n"
        "v_mfma_f32_16x16x32_bf16 v[80:83], v[144:147], a[200:203], v[80:83]\n"
        "v_mfma_f32_16x16x32_bf16 v[80:83], v[148:151], a[216:219], v[80:83]\n"
        "v_mfma_f32_16x16x32_bf16 v[84:87], v[144:147], a[204:207], v[84:87]\n"
        "v_mfma_f32_16x16x32_bf16 v[84:87], v[148:151], a[220:223], v[84:87]\n"
        "s_waitcnt vmcnt(15)\n"
        "v_mfma_f32_16x16x32_bf16 v[72:75], v[152:155], a[224:227], v[72:75]\n"
        "global_load_dwordx4 v[144:147], v198, s[78:79] offset:0 sc1\n"
        "global_load_dwordx4 v[148:151], v198, s[78:79] offset:1024 sc1\n"
        "v_mfma_f32_16x16x32_bf16 v[72:75], v[156:159], a[240:243], v[72:75]\n"
        "v_mfma_f32_16x16x32_bf16 v[76:79], v[152:155], a[228:231], v[76:79]\n"
        "v_mfma_f32_16x16x32_bf16 v[76:79], v[156:159], a[244:247], v[76:79]\n"
        "v_mfma_f32_16x16x32_bf16 v[80:83], v[152:155], a[232:235], v[80:83]\n"
        "v_mfma_f32_16x16x32_bf16 v[80:83], v[156:159], a[248:251], v[80:83]\n"
        "v_mfma_f32_16x16x32_bf16 v[84:87], v[152:155], a[236:239], v[84:87]\n"
        "v_mfma_f32_16x16x32_bf16 v[84:87], v[156:159], a[252:255], v[84:87]\n"
        "s_waitcnt vmcnt(12)\n"
        "v_mfma_f32_16x16x32_bf16 v[56:59], v[96:99], a[0:3], 0\n"
        "global_load_dwordx4 v[152:155], v199, s[78:79] offset:0 sc1\n"
        "global_load_dwordx4 v[156:159], v199, s[78:79] offset:1024 sc1\n"
        "v_mfma_f32_16x16x32_bf16 v[56:59], v[100:103], a[16:19], v[56:59]\n"
        "v_mfma_f32_16x16x32_bf16 v[60:63], v[96:99], a[4:7], 0\n"
        "v_mfma_f32_16x16x32_bf16 v[60:63], v[100:103], a[20:23], v[60:63]\n"
        "v_mfma_f32_16x16x32_bf16 v[64:67], v[96:99], a[8:11], 0\n"
        "ds_write_b128 v47, v[76:79] offset:3072\n"
        "ds_write_b128 v47, v[80:83] offset:4096\n"
        "v_mfma_f32_16x16x32_bf16 v[64:67], v[100:103], a[24:27], v[64:67]\n"
        "ds_write_b128 v47, v[84:87] offset:5120\n"
        "v_mfma_f32_16x16x32_bf16 v[68:71], v[96:99], a[12:15], 0\n"
        "v_mfma_f32_16x16x32_bf16 v[68:71], v[100:103], a[28:31], v[68:71]\n"
        "s_waitcnt vmcnt(12)\n"
        "v_mfma_f32_16x16x32_bf16 v[56:59], v[104:107], a[32:35], v[56:59]\n"
        "s_mov_b32 s53, 0x40000\n"
        "Lib_18:\n"
        "v_max3_u32 v54, v240, v241, v242\n"
        "v_max3_u32 v54, v54, v243, v244\n"
        "v_max3_u32 v54, v54, v245, v246\n"
        "v_max3_u32 v54, v54, v247, v248\n"
        "v_max_u32 v54, v54, v249\n"
        "v_cmp_eq_u32 vcc, -1, v54\n"
        "s_nop 4\n"
        "s_cbranch_vccz Libok_19\n"
        "global_load_dwordx4 v[240:243], v44, s[38:39] offset:0 sc1\n"
        "global_load_dwordx4 v[244:247], v44, s[38:39] offset:1024 sc1\n"
        "global_load_dwordx2 v[248:249], v44, s[38:39] offset:2048 sc1\n"
        "s_waitcnt vmcnt(0)\n"
        "s_sub_u32 s53, s53, 1\n"
        "s_cbranch_scc0 Lib_18\n"
        "Libok_19:\n"
        "v_mfma_f32_16x16x32_bf16 v[56:59], v[108:111], a[48:51], v[56:59]\n"
        "v_mfma_f32_16x16x32_bf16 v[60:63], v[104:107], a[36:39], v[60:63]\n"
        "v_mfma_f32_16x16x32_bf16 v[60:63], v[108:111], a[52:55], v[60:63]\n"
        "v_mfma_f32_16x16x32_bf16 v[64:67], v[104:107], a[40:43], v[64:67]\n"
        "s_waitcnt lgkmcnt(0)\n"
        "s_barrier\n"
        "v_mfma_f32_16x16x32_bf16 v[64:67], v[108:111], a[56:59], v[64:67]\n"
        "ds_read_b128 v[4:7], v48 offset:3072\n"
        "ds_read_b128 v[8:11], v49 offset:3072\n"
        "v_mfma_f32_16x16x32_bf16 v[68:71], v[104:107], a[44:47], v[68:71]\n"
        "ds_read_b128 v[12:15], v50 offset:3072\n"
        "v_lshlrev_b32 v54, 16, v240\n"
        "v_mfma_f32_16x16x32_bf16 v[68:71], v[108:111], a[60:63], v[68:71]\n"
        "v_and_b32 v240, 0xffff0000, v240\n"
        "v_lshlrev_b32 v55, 16, v241\n"
        "v_and_b32 v241, 0xffff0000, v241\n"
        "s_waitcnt vmcnt(10)\n"
        "v_mfma_f32_16x16x32_bf16 v[56:59], v[112:115], a[64:67], v[56:59]\n"
        "v_sub_f32 v208, v208, v54\n"
        "v_sub_f32 v209, v209, v240\n"
        "v_mfma_f32_16x16x32_bf16 v[56:59], v[116:119], a[80:83], v[56:59]\n"
        "v_sub_f32 v210, v210, v55\n"
        "v_sub_f32 v211, v211, v241\n"
        "v_mfma_f32_16x16x32_bf16 v[60:63], v[112:115], a[68:71], v[60:63]\n"
        "v_mfma_f32_16x16x32_bf16 v[60:63], v[116:119], a[84:87], v[60:63]\n"
        "v_mfma_f32_16x16x32_bf16 v[64:67], v[112:115], a[72:75], v[64:67]\n"
        "s_waitcnt lgkmcnt(0)\n"
        "v_add_f32 v4, v4, v8\n"
        "v_mfma_f32_16x16x32_bf16 v[64:67], v[116:119], a[88:91], v[64:67]\n"
        "v_add_f32 v5, v5, v9\n"
        "v_add_f32 v6, v6, v10\n"
        "v_mfma_f32_16x16x32_bf16 v[68:71], v[112:115], a[76:79], v[68:71]\n"
        "v_add_f32 v7, v7, v11\n"
        "v_add_f32 v220, v72, v12\n"
        "v_mfma_f32_16x16x32_bf16 v[68:71], v[116:119], a[92:95], v[68:71]\n"
        "v_add_f32 v221, v73, v13\n"
        "v_cvt_pk_bf16_f32 v208, v208, v209\n"
        "v_cvt_pk_bf16_f32 v209, v210, v211\n"
        "v_lshlrev_b32 v54, 16, v242\n"
        "v_and_b32 v242, 0xffff0000, v242\n"
        "s_waitcnt vmcnt(8)\n"
        "v_mfma_f32_16x16x32_bf16 v[56:59], v[120:123], a[96:99], v[56:59]\n"
        "v_add_f32 v222, v74, v14\n"
        "v_add_f32 v223, v75, v15\n"
        "v_mfma_f32_16x16x32_bf16 v[56:59], v[124:127], a[112:115], v[56:59]\n"
        "v_add_f32 v220, v220, v4\n"
        "v_add_f32 v221, v221, v5\n"
        "v_mfma_f32_16x16x32_bf16 v[60:63], v[120:123], a[100:103], v[60:63]\n"
        "v_add_f32 v222, v222, v6\n"
        "v_add_f32 v223, v223, v7\n"
        "v_mfma_f32_16x16x32_bf16 v[60:63], v[124:127], a[116:119], v[60:63]\n"
        "v_lshlrev_b32 v55, 16, v243\n"
        "v_and_b32 v243, 0xffff0000, v243\n"
        "v_sub_f32 v212, v212, v54\n"
        "v_sub_f32 v213, v213, v242\n"
        "v_mfma_f32_16x16x32_bf16 v[64:67], v[120:123], a[104:107], v[64:67]\n"
        "v_sub_f32 v214, v214, v55\n"
        "v_sub_f32 v215, v215, v243\n"
        "v_mfma_f32_16x16x32_bf16 v[64:67], v[124:127], a[120:123], v[64:67]\n"
        "v_cvt_pk_bf16_f32 v212, v212, v213\n"
        "v_cvt_pk_bf16_f32 v213, v214, v215\n"
        "v_mfma_f32_16x16x32_bf16 v[68:71], v[120:123], a[108:111], v[68:71]\n"
        "v_mfma_f32_16x16x32_bf16 v[68:71], v[124:127], a[124:127], v[68:71]\n"
        "s_waitcnt vmcnt(6)\n"
        "v_mfma_f32_16x16x32_bf16 v[60:63], v[128:131], a[132:135], v[60:63]\n"
        "v_mov_b32 v210, v212\n"
        "v_mov_b32 v211, v213\n"
        "s_nop 1\n"
        "v_mfma_f32_16x16x32_bf16 v[60:63], v[132:135], a[148:151], v[60:63]\n"
        "v_permlane32_swap_b32 v208, v210\n"
        "v_permlane32_swap_b32 v209, v211\n"
        "s_nop 1\n"
        "v_permlane16_swap_b32 v208, v210\n"
        "v_permlane16_swap_b32 v209, v211\n"
        "s_nop 1\n"
        "v_mfma_f32_16x16x32_bf16 v[64:67], v[128:131], a[136:139], v[64:67]\n"
        "global_store_dwordx4 v52, v[208:211], s[34:35] offset:0 sc1\n"
        "v_lshlrev_b32 v54, 16, v244\n"
        "v_mfma_f32_16x16x32_bf16 v[64:67], v[132:135], a[152:155], v[64:67]\n"
        "v_and_b32 v244, 0xffff0000, v244\n"
        "v_lshlrev_b32 v55, 16, v245\n"
        "v_mfma_f32_16x16x32_bf16 v[68:71], v[128:131], a[140:143], v[68:71]\n"
        "v_mfma_f32_16x16x32_bf16 v[68:71], v[132:135], a[156:159], v[68:71]\n"
        "s_waitcnt vmcnt(5)\n"
        "v_mfma_f32_16x16x32_bf16 v[60:63], v[136:139], a[164:167], v[60:63]\n"
        "v_and_b32 v245, 0xffff0000, v245\n"
        "v_sub_f32 v216, v216, v54\n"
        "v_mfma_f32_16x16x32_bf16 v[60:63], v[140:143], a[180:183], v[60:63]\n"
        "v_sub_f32 v217, v217, v244\n"
        "v_sub_f32 v218, v218, v55\n"
        "v_mfma_f32_16x16x32_bf16 v[64:67], v[136:139], a[168:171], v[64:67]\n"
        "v_sub_f32 v219, v219, v245\n"
        "v_cvt_pk_bf16_f32 v216, v216, v217\n"
        "v_mfma_f32_16x16x32_bf16 v[64:67], v[140:143], a[184:187], v[64:67]\n"
        "v_cvt_pk_bf16_f32 v217, v218, v219\n"
        "v_lshlrev_b32 v54, 16, v246\n"
        "v_mfma_f32_16x16x32_bf16 v[68:71], v[136:139], a[172:175], v[68:71]\n"
        "v_mfma_f32_16x16x32_bf16 v[68:71], v[140:143], a[188:191], v[68:71]\n"
        "s_waitcnt vmcnt(3)\n"
        "v_mfma_f32_16x16x32_bf16 v[60:63], v[144:147], a[196:199], v[60:63]\n"
        "v_and_b32 v246, 0xffff0000, v246\n"
        "v_lshlrev_b32 v55, 16, v247\n"
        "v_mfma_f32_16x16x32_bf16 v[60:63], v[148:151], a[212:215], v[60:63]\n"
        "v_and_b32 v247, 0xffff0000, v247\n"
        "v_sub_f32 v220, v220, v54\n"
        "v_mfma_f32_16x16x32_bf16 v[64:67], v[144:147], a[200:203], v[64:67]\n"
        "v_sub_f32 v221, v221, v246\n"
        "v_sub_f32 v222, v222, v55\n"
        "v_mfma_f32_16x16x32_bf16 v[64:67], v[148:151], a[216:219], v[64:67]\n"
        "v_sub_f32 v223, v223, v247\n"
        "v_cvt_pk_bf16_f32 v220, v220, v221\n"
        "v_mfma_f32_16x16x32_bf16 v[68:71], v[144:147], a[204:207], v[68:71]\n"
        "v_mfma_f32_16x16x32_bf16 v[68:71], v[148:151], a[220:223], v[68:71]\n"
        "s_waitcnt vmcnt(1)\n"
        "v_mfma_f32_16x16x32_bf16 v[60:63], v[152:155], a[228:231], v[60:63]\n"
        "v_cvt_pk_bf16_f32 v221, v222, v223\n"
        "v_mov_b32 v218, v220\n"
        "v_mfma_f32_16x16x32_bf16 v[60:63], v[156:159], a[244:247], v[60:63]\n"
        "v_mov_b32 v219, v221\n"
        "s_nop 1\n"
        "v_permlane32_swap_b32 v216, v218\n"
        "v_permlane32_swap_b32 v217, v219\n"
        "s_nop 1\n"
        "v_mfma_f32_16x16x32_bf16 v[64:67], v[152:155], a[232:235], v[64:67]\n"
        "v_permlane16_swap_b32 v216, v218\n"
        "v_permlane16_swap_b32 v217, v219\n"
        "s_nop 1\n"
        "global_store_dwordx4 v52, v[216:219], s[34:35] offset:64 sc1\n"
        "v_mfma_f32_16x16x32_bf16 v[64:67], v[156:159], a[248:251], v[64:67]\n"
        "v_mfma_f32_16x16x32_bf16 v[68:71], v[152:155], a[236:239], v[68:71]\n"
        "v_mfma_f32_16x16x32_bf16 v[68:71], v[156:159], a[252:255], v[68:71]\n"
        "v_mfma_f32_16x16x32_bf16 v[56:59], v[128:131], a[128:131], v[56:59]\n"
        "v_lshlrev_b32 v224, 16, v248\n"
        "v_and_b32 v225, 0xffff0000, v248\n"
        "v_mfma_f32_16x16x32_bf16 v[56:59], v[132:135], a[144:147], v[56:59]\n"
        "v_lshlrev_b32 v226, 16, v249\n"
        "v_and_b32 v227, 0xffff0000, v249\n"
        "v_mfma_f32_16x16x32_bf16 v[56:59], v[136:139], a[160:163], v[56:59]\n"
        "s_nop 0\n"
        "ds_write_b128 v47, v[60:63] offset:0\n"
        "ds_write_b128 v47, v[64:67] offset:1024\n"
        "v_mfma_f32_16x16x32_bf16 v[56:59], v[140:143], a[176:179], v[56:59]\n"
        "ds_write_b128 v47, v[68:71] offset:2048\n"
        "v_mfma_f32_16x16x32_bf16 v[56:59], v[144:147], a[192:195], v[56:59]\n"
        "v_mfma_f32_16x16x32_bf16 v[56:59], v[148:151], a[208:211], v[56:59]\n"
        "v_mfma_f32_16x16x32_bf16 v[56:59], v[152:155], a[224:227], v[56:59]\n"
        "s_waitcnt lgkmcnt(0)\n"
        "s_barrier\n"
        "ds_read_b128 v[4:7], v48 offset:0\n"
        "ds_read_b128 v[8:11], v49 offset:0\n"
        "ds_read_b128 v[12:15], v50 offset:0\n"
        "v_mfma_f32_16x16x32_bf16 v[56:59], v[156:159], a[240:243], v[56:59]\n"
        "s_waitcnt lgkmcnt(0)\n"
        "v_add_f32 v4, v4, v8\n"
        "v_add_f32 v5, v5, v9\n"
        "v_add_f32 v6, v6, v10\n"
        "v_add_f32 v7, v7, v11\n"
        "v_sub_f32 v224, v12, v224\n"
        "v_sub_f32 v225, v13, v225\n"
        "v_sub_f32 v226, v14, v226\n"
        "v_sub_f32 v227, v15, v227\n"
        "v_add_f32 v224, v224, v4\n"
        "v_add_f32 v225, v225, v5\n"
        "v_add_f32 v226, v226, v6\n"
        "v_add_f32 v227, v227, v7\n"
        "v_add_f32 v224, v224, v56\n"
        "v_add_f32 v225, v225, v57\n"
        "v_add_f32 v226, v226, v58\n"
        "v_add_f32 v227, v227, v59\n"
        "v_cvt_pk_bf16_f32 v224, v224, v225\n"
        "v_cvt_pk_bf16_f32 v225, v226, v227\n"
        "global_store_dwordx2 v38, v[224:225], s[34:35] offset:128 sc1\n"
        "s_endpgm\n"
        "Lh1:\n"
        "global_load_dwordx4 v[96:99], v192, s[78:79] offset:0 sc1\n"
        "global_load_dwordx4 v[100:103], v192, s[78:79] offset:1024 sc1\n"
        "global_load_dwordx4 v[104:107], v193, s[78:79] offset:0 sc1\n"
        "global_load_dwordx4 v[108:111], v193, s[78:79] offset:1024 sc1\n"
        "global_load_dwordx4 v[112:115], v194, s[78:79] offset:0 sc1\n"
        "global_load_dwordx4 v[116:119], v194, s[78:79] offset:1024 sc1\n"
        "global_load_dwordx4 v[120:123], v195, s[78:79] offset:0 sc1\n"
        "global_load_dwordx4 v[124:127], v195, s[78:79] offset:1024 sc1\n"
        "global_load_dwordx4 v[128:131], v196, s[78:79] offset:0 sc1\n"
        "global_load_dwordx4 v[132:135], v196, s[78:79] offset:1024 sc1\n"
        "global_load_dwordx4 v[136:139], v197, s[78:79] offset:0 sc1\n"
        "global_load_dwordx4 v[140:143], v197, s[78:79] offset:1024 sc1\n"
        "global_load_dwordx4 v[144:147], v198, s[78:79] offset:0 sc1\n"
        "global_load_dwordx4 v[148:151], v198, s[78:79] offset:1024 sc1\n"
        "s_waitcnt vmcnt(24)\n"
        "ds_read_b128 a[200:203], v28 offset:10240\n"
        "ds_read_b128 a[216:219], v29 offset:10240\n"
        "s_waitcnt vmcnt(22)\n"
        "ds_read_b128 a[232:235], v28 offset:12288\n"
        "ds_read_b128 a[248:251], v29 offset:12288\n"
        "s_waitcnt vmcnt(20)\n"
        "ds_read_b128 a[140:143], v28 offset:0\n"
        "ds_read_b128 a[156:159], v29 offset:0\n"
        "s_waitcnt vmcnt(18)\n"
        "ds_read_b128 a[172:175], v28 offset:2048\n"
        "ds_read_b128 a[188:191], v29 offset:2048\n"
        "s_waitcnt vmcnt(16)\n"
        "ds_read_b128 a[204:207], v28 offset:4096\n"
        "ds_read_b128 a[220:223], v29 offset:4096\n"
        "s_waitcnt vmcnt(14)\n"
        "ds_read_b128 a[236:239], v28 offset:6144\n"
        "ds_read_b128 a[252:255], v29 offset:6144\n"
        "s_waitcnt lgkmcnt(0)\n"
        "s_waitcnt vmcnt(12)\n"
        "v_mfma_f32_16x16x32_bf16 v[56:59], v[96:99], a[0:3], 0\n"
        "global_load_dwordx4 v[152:155], v199, s[78:79] offset:0 sc1\n"
        "global_load_dwordx4 v[156:159], v199, s[78:79] offset:1024 sc1\n"
        "s_add_u32 s78, s78, 0x10080\n"
        "s_addc_u32 s79, s79, 0\n"
        "v_mfma_f32_16x16x32_bf16 v[56:59], v[100:103], a[16:19], v[56:59]\n"
        "v_mfma_f32_16x16x32_bf16 v[60:63], v[96:99], a[4:7], 0\n"
        "v_mfma_f32_16x16x32_bf16 v[60:63], v[100:103], a[20:23], v[60:63]\n"
        "v_mfma_f32_16x16x32_bf16 v[64:67], v[96:99], a[8:11], 0\n"
        "v_mfma_f32_16x16x32_bf16 v[64:67], v[100:103], a[24:27], v[64:67]\n"
        "v_mfma_f32_16x16x32_bf16 v[68:71], v[96:99], a[12:15], 0\n"
        "v_mfma_f32_16x16x32_bf16 v[68:71], v[100:103], a[28:31], v[68:71]\n"
        "s_waitcnt vmcnt(12)\n"
        "v_mfma_f32_16x16x32_bf16 v[56:59], v[104:107], a[32:35], v[56:59]\n"
        "global_load_dwordx4 v[96:99], v192, s[78:79] offset:0 sc1\n"
        "global_load_dwordx4 v[100:103], v192, s[78:79] offset:1024 sc1\n"
        "v_mfma_f32_16x16x32_bf16 v[56:59], v[108:111], a[48:51], v[56:59]\n"
        "v_mfma_f32_16x16x32_bf16 v[60:63], v[104:107], a[36:39], v[60:63]\n"
        "v_mfma_f32_16x16x32_bf16 v[60:63], v[108:111], a[52:55], v[60:63]\n"
        "v_mfma_f32_16x16x32_bf16 v[64:67], v[104:107], a[40:43], v[64:67]\n"
        "v_mfma_f32_16x16x32_bf16 v[64:67], v[108:111], a[56:59], v[64:67]\n"
        "v_mfma_f32_16x16x32_bf16 v[68:71], v[104:107], a[44:47], v[68:71]\n"
        "v_mfma_f32_16x16x32_bf16 v[68:71], v[108:111], a[60:63], v[68:71]\n"
        "s_waitcnt vmcnt(12)\n"
        "v_mfma_f32_16x16x32_bf16 v[56:59], v[112:115], a[64:67], v[56:59]\n"
        "global_load_dwordx4 v[104:107], v193, s[78:79] offset:0 sc1\n"
        "global_load_dwordx4 v[108:111], v193, s[78:79] offset:1024 sc1\n"
        "v_mfma_f32_16x16x32_bf16 v[56:59], v[116:119], a[80:83], v[56:59]\n"
        "v_mfma_f32_16x16x32_bf16 v[60:63], v[112:115], a[68:71], v[60:63]\n"
        "v_mfma_f32_16x16x32_bf16 v[60:63], v[116:119], a[84:87], v[60:63]\n"
        "v_mfma_f32_16x16x32_bf16 v[64:67], v[112:115], a[72:75], v[64:67]\n"
        "v_mfma_f32_16x16x32_bf16 v[64:67], v[116:119], a[88:91], v[64:67]\n"
        "v_mfma_f32_16x16x32_bf16 v[68:71], v[112:115], a[76:79], v[68:71]\n"
        "v_mfma_f32_16x16x32_bf16 v[68:71], v[116:119], a[92:95], v[68:71]\n"
        "s_waitcnt vmcnt(12)\n"
        "v_mfma_f32_16x16x32_bf16 v[56:59], v[120:123], a[96:99], v[56:59]\n"
        "global_load_dwordx4 v[112:115], v194, s[78:79] offset:0 sc1\n"
        "global_load_dwordx4 v[116:119], v194, s[78:79] offset:1024 sc1\n"
        "v_mfma_f32_16x16x32_bf16 v[56:59], v[124:127], a[112:115], v[56:59]\n"
        "v_mfma_f32_16x16x32_bf16 v[60:63], v[120:123], a[100:103], v[60:63]\n"
        "v_mfma_f32_16x16x32_bf16 v[60:63], v[124:127], a[116:119], v[60:63]\n"
        "v_mfma_f32_16x16x32_bf16 v[64:67], v[120:123], a[104:107], v[64:67]\n"
        "v_mfma_f32_16x16x32_bf16 v[64:67], v[124:127], a[120:123], v[64:67]\n"
        "v_mfma_f32_16x16x32_bf16 v[68:71], v[120:123], a[108:111], v[68:71]\n"
        "v_mfma_f32_16x16x32_bf16 v[68:71], v[124:127], a[124:127], v[68:71]\n"
        "s_waitcnt vmcnt(12)\n"
        "v_mfma_f32_16x16x32_bf16 v[56:59], v[128:131], a[128:131], v[56:59]\n"
        "global_load_dwordx4 v[120:123], v195, s[78:79] offset:0 sc1\n"
        "global_load_dwordx4 v[124:127], v195, s[78:79] offset:1024 sc1\n"
        "v_mfma_f32_16x16x32_bf16 v[56:59], v[132:135], a[144:147], v[56:59]\n"
        "v_mfma_f32_16x16x32_bf16 v[60:63], v[128:131], a[132:135], v[60:63]\n"
        "v_mfma_f32_16x16x32_bf16 v[60:63], v[132:135], a[148:151], v[60:63]\n"
        "v_mfma_f32_16x16x32_bf16 v[64:67], v[128:131], a[136:139], v[64:67]\n"
        "v_mfma_f32_16x16x32_bf16 v[64:67], v[132:135], a[152:155], v[64:67]\n"
        "v_mfma_f32_16x16x32_bf16 v[68:71], v[128:131], a[140:143], v[68:71]\n"
        "v_mfma_f32_16x16x32_bf16 v[68:71], v[132:135], a[156:159], v[68:71]\n"
        "s_waitcnt vmcnt(12)\n"
        "v_mfma_f32_16x16x32_bf16 v[56:59], v[136:139], a[160:163], v[56:59]\n"
        "global_load_dwordx4 v[128:131], v196, s[78:79] offset:0 sc1\n"
        "global_load_dwordx4 v[132:135], v196, s[78:79] offset:1024 sc1\n"
        "v_mfma_f32_16x16x32_bf16 v[56:59], v[140:143], a[176:179], v[56:59]\n"
        "v_mfma_f32_16x16x32_bf16 v[60:63], v[136:139], a[164:167], v[60:63]\n"
        "v_mfma_f32_16x16x32_bf16 v[60:63], v[140:143], a[180:183], v[60:63]\n"
        "v_mfma_f32_16x16x32_bf16 v[64:67], v[136:139], a[168:171], v[64:67]\n"
        "v_mfma_f32_16x16x32_bf16 v[64:67], v[140:143], a[184:187], v[64:67]\n"
        "v_mfma_f32_16x16x32_bf16 v[68:71], v[136:139], a[172:175], v[68:71]\n"
        "v_mfma_f32_16x16x32_bf16 v[68:71], v[140:143], a[188:191], v[68:71]\n"
        "s_waitcnt vmcnt(12)\n"
        "v_mfma_f32_16x16x32_bf16 v[56:59], v[144:147], a[192:195], v[56:59]\n"
        "global_load_dwordx4 v[136:139], v197, s[78:79] offset:0 sc1\n"
        "global_load_dwordx4 v[140:143], v197, s[78:79] offset:1024 sc1\n"
        "v_mfma_f32_16x16x32_bf16 v[56:59], v[148:151], a[208:211], v[56:59]\n"
        "v_mfma_f32_16x16x32_bf16 v[60:63], v[144:147], a[196:199], v[60:63]\n"
        "v_mfma_f32_16x16x32_bf16 v[60:63], v[148:151], a[212:215], v[60:63]\n"
        "v_mfma_f32_16x16x32_bf16 v[64:67], v[144:147], a[200:203], v[64:67]\n"
        "v_mfma_f32_16x16x32_bf16 v[64:67], v[148:151], a[216:219], v[64:67]\n"
        "v_mfma_f32_16x16x32_bf16 v[68:71], v[144:147], a[204:207], v[68:71]\n"
        "v_mfma_f32_16x16x32_bf16 v[68:71], v[148:151], a[220:223], v[68:71]\n"
        "s_waitcnt vmcnt(12)\n"
        "v_mfma_f32_16x16x32_bf16 v[56:59], v[152:155], a[224:227], v[56:59]\n"
        "global_load_dwordx4 v[144:147], v198, s[78:79] offset:0 sc1\n"
        "global_load_dwordx4 v[148:151], v198, s[78:79] offset:1024 sc1\n"
        "v_mfma_f32_16x16x32_bf16 v[56:59], v[156:159], a[240:243], v[56:59]\n"
        "v_mfma_f32_16x16x32_bf16 v[60:63], v[152:155], a[228:231], v[60:63]\n"
        "v_mfma_f32_16x16x32_bf16 v[60:63], v[156:159], a[244:247], v[60:63]\n"
        "v_mfma_f32_16x16x32_bf16 v[64:67], v[152:155], a[232:235], v[64:67]\n"
        "v_mfma_f32_16x16x32_bf16 v[64:67], v[156:159], a[248:251], v[64:67]\n"
        "v_mfma_f32_16x16x32_bf16 v[68:71], v[152:155], a[236:239], v[68:71]\n"
        "v_mfma_f32_16x16x32_bf16 v[68:71], v[156:159], a[252:255], v[68:71]\n"
        "s_waitcnt vmcnt(12)\n"
        "v_mfma_f32_16x16x32_bf16 v[72:75], v[96:99], a[0:3], 0\n"
        "global_load_dwordx4 v[152:155], v199, s[78:79] offset:0 sc1\n"
        "global_load_dwordx4 v[156:159], v199, s[78:79] offset:1024 sc1\n"
        "s_add_u32 s78, s78, 0x10080\n"
        "s_addc_u32 s79, s79, 0\n"
        "v_mfma_f32_16x16x32_bf16 v[72:75], v[100:103], a[16:19], v[72:75]\n"
        "v_mfma_f32_16x16x32_bf16 v[76:79], v[96:99], a[4:7], 0\n"
        "v_mfma_f32_16x16x32_bf16 v[76:79], v[100:103], a[20:23], v[76:79]\n"
        "v_mfma_f32_16x16x32_bf16 v[80:83], v[96:99], a[8:11], 0\n"
        "ds_write_b128 v47, v[60:63] offset:0\n"
        "ds_write_b128 v47, v[64:67] offset:1024\n"
        "v_mfma_f32_16x16x32_bf16 v[80:83], v[100:103], a[24:27], v[80:83]\n"
        "ds_write_b128 v47, v[68:71] offset:2048\n"
        "v_mfma_f32_16x16x32_bf16 v[84:87], v[96:99], a[12:15], 0\n"
        "v_mfma_f32_16x16x32_bf16 v[84:87], v[100:103], a[28:31], v[84:87]\n"
        "s_waitcnt vmcnt(12)\n"
        "v_mfma_f32_16x16x32_bf16 v[72:75], v[104:107], a[32:35], v[72:75]\n"
        "global_load_dwordx4 v[96:99], v192, s[78:79] offset:0 sc1\n"
        "global_load_dwordx4 v[100:103], v192, s[78:79] offset:1024 sc1\n"
        "v_mfma_f32_16x16x32_bf16 v[72:75], v[108:111], a[48:51], v[72:75]\n"
        "v_mfma_f32_16x16x32_bf16 v[76:79], v[104:107], a[36:39], v[76:79]\n"
        "v_mfma_f32_16x16x32_bf16 v[76:79], v[108:111], a[52:55], v[76:79]\n"
        "v_mfma_f32_16x16x32_bf16 v[80:83], v[104:107], a[40:43], v[80:83]\n"
        "s_waitcnt lgkmcnt(0)\n"
        "s_barrier\n"
        "v_mfma_f32_16x16x32_bf16 v[80:83], v[108:111], a[56:59], v[80:83]\n"
        "ds_read_b128 v[4:7], v48 offset:0\n"
        "ds_read_b128 v[8:11], v49 offset:0\n"
        "v_mfma_f32_16x16x32_bf16 v[84:87], v[104:107], a[44:47], v[84:87]\n"
        "ds_read_b128 v[12:15], v50 offset:0\n"
        "v_mfma_f32_16x16x32_bf16 v[84:87], v[108:111], a[60:63], v[84:87]\n"
        "s_waitcnt vmcnt(12)\n"
        "v_mfma_f32_16x16x32_bf16 v[72:75], v[112:115], a[64:67], v[72:75]\n"
        "global_load_dwordx4 v[104:107], v193, s[78:79] offset:0 sc1\n"
        "global_load_dwordx4 v[108:111], v193, s[78:79] offset:1024 sc1\n"
        "v_mfma_f32_16x16x32_bf16 v[72:75], v[116:119], a[80:83], v[72:75]\n"
        "global_load_dword v36, v32, s[30:31] sc1\n"
        "v_mfma_f32_16x16x32_bf16 v[76:79], v[112:115], a[68:71], v[76:79]\n"
        "v_mfma_f32_16x16x32_bf16 v[76:79], v[116:119], a[84:87], v[76:79]\n"
        "v_mfma_f32_16x16x32_bf16 v[80:83], v[112:115], a[72:75], v[80:83]\n"
        "s_waitcnt lgkmcnt(0)\n"
        "v_add_f32 v4, v4, v8\n"
        "v_mfma_f32_16x16x32_bf16 v[80:83], v[116:119], a[88:91], v[80:83]\n"
        "v_add_f32 v5, v5, v9\n"
        "v_add_f32 v6, v6, v10\n"
        "v_mfma_f32_16x16x32_bf16 v[84:87], v[112:115], a[76:79], v[84:87]\n"
        "v_add_f32 v7, v7, v11\n"
        "v_add_f32 v12, v56, v12\n"
        "v_mfma_f32_16x16x32_bf16 v[84:87], v[116:119], a[92:95], v[84:87]\n"
        "v_add_f32 v13, v57, v13\n"
        "s_waitcnt vmcnt(13)\n"
        "v_mfma_f32_16x16x32_bf16 v[72:75], v[120:123], a[96:99], v[72:75]\n"
        "global_load_dwordx4 v[112:115], v194, s[78:79] offset:0 sc1\n"
        "global_load_dwordx4 v[116:119], v194, s[78:79] offset:1024 sc1\n"
        "v_mfma_f32_16x16x32_bf16 v[72:75], v[124:127], a[112:115], v[72:75]\n"
        "v_add_f32 v14, v58, v14\n"
        "v_add_f32 v15, v59, v15\n"
        "v_mfma_f32_16x16x32_bf16 v[76:79], v[120:123], a[100:103], v[76:79]\n"
        "v_add_f32 v12, v12, v4\n"
        "v_add_f32 v13, v13, v5\n"
        "v_mfma_f32_16x16x32_bf16 v[76:79], v[124:127], a[116:119], v[76:79]\n"
        "v_add_f32 v14, v14, v6\n"
        "v_add_f32 v15, v15, v7\n"
        "v_mfma_f32_16x16x32_bf16 v[80:83], v[120:123], a[104:107], v[80:83]\n"
        "v_cvt_pk_bf16_f32 v228, v12, v13\n"
        "v_cvt_pk_bf16_f32 v229, v14, v15\n"
        "v_mfma_f32_16x16x32_bf16 v[80:83], v[124:127], a[120:123], v[80:83]\n"
        "v_min_u32 v228, -2, v228\n"
        "v_min_u32 v229, -2, v229\n"
        "v_mfma_f32_16x16x32_bf16 v[84:87], v[120:123], a[108:111], v[84:87]\n"
        "v_mfma_f32_16x16x32_bf16 v[84:87], v[124:127], a[124:127], v[84:87]\n"
        "s_waitcnt vmcnt(13)\n"
        "v_mfma_f32_16x16x32_bf16 v[72:75], v[128:131], a[128:131], v[72:75]\n"
        "global_load_dwordx4 v[120:123], v195, s[78:79] offset:0 sc1\n"
        "global_load_dwordx4 v[124:127], v195, s[78:79] offset:1024 sc1\n"
        "v_mfma_f32_16x16x32_bf16 v[72:75], v[132:135], a[144:147], v[72:75]\n"
        "v_mfma_f32_16x16x32_bf16 v[76:79], v[128:131], a[132:135], v[76:79]\n"
        "v_mfma_f32_16x16x32_bf16 v[76:79], v[132:135], a[148:151], v[76:79]\n"
        "v_mfma_f32_16x16x32_bf16 v[80:83], v[128:131], a[136:139], v[80:83]\n"
        "v_mfma_f32_16x16x32_bf16 v[80:83], v[132:135], a[152:155], v[80:83]\n"
        "v_mfma_f32_16x16x32_bf16 v[84:87], v[128:131], a[140:143], v[84:87]\n"
        "v_mfma_f32_16x16x32_bf16 v[84:87], v[132:135], a[156:159], v[84:87]\n"
        "s_waitcnt vmcnt(13)\n"
        "v_mfma_f32_16x16x32_bf16 v[72:75], v[136:139], a[160:163], v[72:75]\n"
        "global_load_dwordx4 v[128:131], v196, s[78:79] offset:0 sc1\n"
        "global_load_dwordx4 v[132:135], v196, s[78:79] offset:1024 sc1\n"
        "v_mfma_f32_16x16x32_bf16 v[72:75], v[140:143], a[176:179], v[72:75]\n"
        "v_mfma_f32_16x16x32_bf16 v[76:79], v[136:139], a[164:167], v[76:79]\n"
        "v_mfma_f32_16x16x32_bf16 v[76:79], v[140:143], a[180:183], v[76:79]\n"
        "v_mfma_f32_16x16x32_bf16 v[80:83], v[136:139], a[168:171], v[80:83]\n"
        "v_mfma_f32_16x16x32_bf16 v[80:83], v[140:143], a[184:187], v[80:83]\n"
        "v_mfma_f32_16x16x32_bf16 v[84:87], v[136:139], a[172:175], v[84:87]\n"
        "v_mfma_f32_16x16x32_bf16 v[84:87], v[140:143], a[188:191], v[84:87]\n"
        "s_waitcnt vmcnt(13)\n"
        "v_mfma_f32_16x16x32_bf16 v[72:75], v[144:147], a[192:195], v[72:75]\n"
        "global_load_dwordx4 v[136:139], v197, s[78:79] offset:0 sc1\n"
        "global_load_dwordx4 v[140:143], v197, s[78:79] offset:1024 sc1\n"
        "v_mfma_f32_16x16x32_bf16 v[72:75], v[148:151], a[208:211], v[72:75]\n"
        "v_mfma_f32_16x16x32_bf16 v[76:79], v[144:147], a[196:199], v[76:79]\n"
        "v_mfma_f32_16x16x32_bf16 v[76:79], v[148:151], a[212:215], v[76:79]\n"
        "v_mfma_f32_16x16x32_bf16 v[80:83], v[144:147], a[200:203], v[80:83]\n"
        "v_mfma_f32_16x16x32_bf16 v[80:83], v[148:151], a[216:219], v[80:83]\n"
        "v_mfma_f32_16x16x32_bf16 v[84:87], v[144:147], a[204:207], v[84:87]\n"
        "v_mfma_f32_16x16x32_bf16 v[84:87], v[148:151], a[220:223], v[84:87]\n"
        "s_waitcnt vmcnt(13)\n"
        "v_mfma_f32_16x16x32_bf16 v[72:75], v[152:155], a[224:227], v[72:75]\n"
        "global_load_dwordx4 v[144:147], v198, s[78:79] offset:0 sc1\n"
        "global_load_dwordx4 v[148:151], v198, s[78:79] offset:1024 sc1\n"
        "v_mfma_f32_16x16x32_bf16 v[72:75], v[156:159], a[240:243], v[72:75]\n"
        "v_mfma_f32_16x16x32_bf16 v[76:79], v[152:155], a[228:231], v[76:79]\n"
        "v_mfma_f32_16x16x32_bf16 v[76:79], v[156:159], a[244:247], v[76:79]\n"
        "v_mfma_f32_16x16x32_bf16 v[80:83], v[152:155], a[232:235], v[80:83]\n"
        "v_mfma_f32_16x16x32_bf16 v[80:83], v[156:159], a[248:251], v[80:83]\n"
        "v_mfma_f32_16x16x32_bf16 v[84:87], v[152:155], a[236:239], v[84:87]\n"
        "v_mfma_f32_16x16x32_bf16 v[84:87], v[156:159], a[252:255], v[84:87]\n"
        "s_waitcnt vmcnt(13)\n"
        "v_mfma_f32_16x16x32_bf16 v[56:59], v[96:99], a[0:3], 0\n"
        "global_load_dwordx4 v[152:155], v199, s[78:79] offset:0 sc1\n"
        "global_load_dwordx4 v[156:159], v199, s[78:79] offset:1024 sc1\n"
        "s_add_u32 s78, s78, 0x10080\n"
        "s_addc_u32 s79, s79, 0\n"
        "v_mfma_f32_16x16x32_bf16 v[56:59], v[100:103], a[16:19], v[56:59]\n"
        "v_mfma_f32_16x16x32_bf16 v[60:63], v[96:99], a[4:7], 0\n"
        "v_mfma_f32_16x16x32_bf16 v[60:63], v[100:103], a[20:23], v[60:63]\n"
        "v_mfma_f32_16x16x32_bf16 v[64:67], v[96:99], a[8:11], 0\n"
        "ds_write_b128 v47, v[76:79] offset:3072\n"
        "ds_write_b128 v47, v[80:83] offset:4096\n"
        "v_mfma_f32_16x16x32_bf16 v[64:67], v[100:103], a[24:27], v[64:67]\n"
        "ds_write_b128 v47, v[84:87] offset:5120\n"
        "v_mfma_f32_16x16x32_bf16 v[68:71], v[96:99], a[12:15], 0\n"
        "v_mfma_f32_16x16x32_bf16 v[68:71], v[100:103], a[28:31], v[68:71]\n"
        "s_waitcnt vmcnt(13)\n"
        "v_mfma_f32_16x16x32_bf16 v[56:59], v[104:107], a[32:35], v[56:59]\n"
        "global_load_dwordx4 v[96:99], v192, s[78:79] offset:0 sc1\n"
        "global_load_dwordx4 v[100:103], v192, s[78:79] offset:1024 sc1\n"
        "v_mfma_f32_16x16x32_bf16 v[56:59], v[108:111], a[48:51], v[56:59]\n"
        "v_mfma_f32_16x16x32_bf16 v[60:63], v[104:107], a[36:39], v[60:63]\n"
        "v_mfma_f32_16x16x32_bf16 v[60:63], v[108:111], a[52:55], v[60:63]\n"
        "v_mfma_f32_16x16x32_bf16 v[64:67], v[104:107], a[40:43], v[64:67]\n"
        "s_waitcnt lgkmcnt(0)\n"
        "s_barrier\n"
        "v_mfma_f32_16x16x32_bf16 v[64:67], v[108:111], a[56:59], v[64:67]\n"
        "ds_read_b128 v[4:7], v48 offset:3072\n"
        "ds_read_b128 v[8:11], v49 offset:3072\n"
        "v_mfma_f32_16x16x32_bf16 v[68:71], v[104:107], a[44:47], v[68:71]\n"
        "ds_read_b128 v[12:15], v50 offset:3072\n"
        "v_mfma_f32_16x16x32_bf16 v[68:71], v[108:111], a[60:63], v[68:71]\n"
        "s_waitcnt vmcnt(12)\n"
        "v_mfma_f32_16x16x32_bf16 v[56:59], v[112:115], a[64:67], v[56:59]\n"
        "global_load_dwordx4 v[104:107], v193, s[78:79] offset:0 sc1\n"
        "global_load_dwordx4 v[108:111], v193, s[78:79] offset:1024 sc1\n"
        "v_mfma_f32_16x16x32_bf16 v[56:59], v[116:119], a[80:83], v[56:59]\n"
        "v_mfma_f32_16x16x32_bf16 v[60:63], v[112:115], a[68:71], v[60:63]\n"
        "v_mfma_f32_16x16x32_bf16 v[60:63], v[116:119], a[84:87], v[60:63]\n"
        "v_mfma_f32_16x16x32_bf16 v[64:67], v[112:115], a[72:75], v[64:67]\n"
        "s_waitcnt lgkmcnt(0)\n"
        "v_add_f32 v4, v4, v8\n"
        "v_mfma_f32_16x16x32_bf16 v[64:67], v[116:119], a[88:91], v[64:67]\n"
        "v_add_f32 v5, v5, v9\n"
        "v_add_f32 v6, v6, v10\n"
        "v_mfma_f32_16x16x32_bf16 v[68:71], v[112:115], a[76:79], v[68:71]\n"
        "v_add_f32 v7, v7, v11\n"
        "v_add_f32 v12, v72, v12\n"
        "v_mfma_f32_16x16x32_bf16 v[68:71], v[116:119], a[92:95], v[68:71]\n"
        "v_add_f32 v13, v73, v13\n"
        "s_waitcnt vmcnt(12)\n"
        "v_mfma_f32_16x16x32_bf16 v[56:59], v[120:123], a[96:99], v[56:59]\n"
        "global_load_dwordx4 v[112:115], v194, s[78:79] offset:0 sc1\n"
        "global_load_dwordx4 v[116:119], v194, s[78:79] offset:1024 sc1\n"
        "v_mfma_f32_16x16x32_bf16 v[56:59], v[124:127], a[112:115], v[56:59]\n"
        "v_add_f32 v14, v74, v14\n"
        "v_add_f32 v15, v75, v15\n"
        "v_mfma_f32_16x16x32_bf16 v[60:63], v[120:123], a[100:103], v[60:63]\n"
        "v_add_f32 v12, v12, v4\n"
        "v_add_f32 v13, v13, v5\n"
        "v_mfma_f32_16x16x32_bf16 v[60:63], v[124:127], a[116:119], v[60:63]\n"
        "v_add_f32 v14, v14, v6\n"
        "v_add_f32 v15, v15, v7\n"
        "v_cmp_ne_u32 vcc, v36, v33\n"
        "s_nop 4\n"
        "s_cbranch_vccz Lrc_21\n"
        "v_mov_b32 v33, v36\n"
        "s_cmp_eq_u32 s43, 0\n"
        "s_cbranch_scc0 Lw_20\n"
        "s_mov_b64 exec, s[22:23]\n"
        "global_store_dword v34, v33, s[30:31] sc1\n"
        "s_mov_b64 exec, -1\n"
        "Lw_20:\n"
        "s_waitcnt vmcnt(0)\n"
        "Lrc_21:\n"
        "v_mfma_f32_16x16x32_bf16 v[64:67], v[120:123], a[104:107], v[64:67]\n"
        "v_cvt_pk_bf16_f32 v230, v12, v13\n"
        "v_cvt_pk_bf16_f32 v231, v14, v15\n"
        "v_mfma_f32_16x16x32_bf16 v[64:67], v[124:127], a[120:123], v[64:67]\n"
        "v_min_u32 v230, -2, v230\n"
        "v_min_u32 v231, -2, v231\n"
        "v_mfma_f32_16x16x32_bf16 v[68:71], v[120:123], a[108:111], v[68:71]\n"
        "v_mfma_f32_16x16x32_bf16 v[68:71], v[124:127], a[124:127], v[68:71]\n"
        "s_waitcnt vmcnt(12)\n"
        "v_mfma_f32_16x16x32_bf16 v[56:59], v[128:131], a[128:131], v[56:59]\n"
        "global_load_dwordx4 v[120:123], v195, s[78:79] offset:0 sc1\n"
        "global_load_dwordx4 v[124:127], v195, s[78:79] offset:1024 sc1\n"
        "v_mfma_f32_16x16x32_bf16 v[56:59], v[132:135], a[144:147], v[56:59]\n"
        "v_mfma_f32_16x16x32_bf16 v[60:63], v[128:131], a[132:135], v[60:63]\n"
        "v_mfma_f32_16x16x32_bf16 v[60:63], v[132:135], a[148:151], v[60:63]\n"
        "v_mfma_f32_16x16x32_bf16 v[64:67], v[128:131], a[136:139], v[64:67]\n"
        "v_mfma_f32_16x16x32_bf16 v[64:67], v[132:135], a[152:155], v[64:67]\n"
        "v_mfma_f32_16x16x32_bf16 v[68:71], v[128:131], a[140:143], v[68:71]\n"
        "v_mfma_f32_16x16x32_bf16 v[68:71], v[132:135], a[156:159], v[68:71]\n"
        "s_waitcnt vmcnt(12)\n"
        "v_mfma_f32_16x16x32_bf16 v[56:59], v[136:139], a[160:163], v[56:59]\n"
        "global_load_dwordx4 v[128:131], v196, s[78:79] offset:0 sc1\n"
        "global_load_dwordx4 v[132:135], v196, s[78:79] offset:1024 sc1\n"
        "v_mfma_f32_16x16x32_bf16 v[56:59], v[140:143], a[176:179], v[56:59]\n"
        "v_mfma_f32_16x16x32_bf16 v[60:63], v[136:139], a[164:167], v[60:63]\n"
        "v_mfma_f32_16x16x32_bf16 v[60:63], v[140:143], a[180:183], v[60:63]\n"
        "v_mfma_f32_16x16x32_bf16 v[64:67], v[136:139], a[168:171], v[64:67]\n"
        "v_mfma_f32_16x16x32_bf16 v[64:67], v[140:143], a[184:187], v[64:67]\n"
        "v_mfma_f32_16x16x32_bf16 v[68:71], v[136:139], a[172:175], v[68:71]\n"
        "v_mfma_f32_16x16x32_bf16 v[68:71], v[140:143], a[188:191], v[68:71]\n"
        "s_waitcnt vmcnt(12)\n"
        "v_mfma_f32_16x16x32_bf16 v[56:59], v[144:147], a[192:195], v[56:59]\n"
        "global_load_dwordx4 v[136:139], v197, s[78:79] offset:0 sc1\n"
        "global_load_dwordx4 v[140:143], v197, s[78:79] offset:1024 sc1\n"
        "v_mfma_f32_16x16x32_bf16 v[56:59], v[148:151], a[208:211], v[56:59]\n"
        "v_mfma_f32_16x16x32_bf16 v[60:63], v[144:147], a[196:199], v[60:63]\n"
        "v_mfma_f32_16x16x32_bf16 v[60:63], v[148:151], a[212:215], v[60:63]\n"
        "v_mfma_f32_16x16x32_bf16 v[64:67], v[144:147], a[200:203], v[64:67]\n"
        "v_mfma_f32_16x16x32_bf16 v[64:67], v[148:151], a[216:219], v[64:67]\n"
        "v_mfma_f32_16x16x32_bf16 v[68:71], v[144:147], a[204:207], v[68:71]\n"
        "v_mfma_f32_16x16x32_bf16 v[68:71], v[148:151], a[220:223], v[68:71]\n"
        "s_waitcnt vmcnt(12)\n"
        "v_mfma_f32_16x16x32_bf16 v[56:59], v[152:155], a[224:227], v[56:59]\n"
        "global_load_dwordx4 v[144:147], v198, s[78:79] offset:0 sc1\n"
        "global_load_dwordx4 v[148:151], v198, s[78:79] offset:1024 sc1\n"
        "v_mfma_f32_16x16x32_bf16 v[56:59], v[156:159], a[240:243], v[56:59]\n"
        "v_mfma_f32_16x16x32_bf16 v[60:63], v[152:155], a[228:231], v[60:63]\n"
        "v_mfma_f32_16x16x32_bf16 v[60:63], v[156:159], a[244:247], v[60:63]\n"
        "v_mfma_f32_16x16x32_bf16 v[64:67], v[152:155], a[232:235], v[64:67]\n"
        "v_mfma_f32_16x16x32_bf16 v[64:67], v[156:159], a[248:251], v[64:67]\n"
        "v_mfma_f32_16x16x32_bf16 v[68:71], v[152:155], a[236:239], v[68:71]\n"
        "v_mfma_f32_16x16x32_bf16 v[68:71], v[156:159], a[252:255], v[68:71]\n"
        "s_waitcnt vmcnt(12)\n"
        "v_mfma_f32_16x16x32_bf16 v[72:75], v[96:99], a[0:3], 0\n"
        "global_load_dwordx4 v[152:155], v199, s[78:79] offset:0 sc1\n"
        "global_load_dwordx4 v[156:159], v199, s[78:79] offset:1024 sc1\n"
        "s_add_u32 s78, s78, 0x10080\n"
        "s_addc_u32 s79, s79, 0\n"
        "v_mfma_f32_16x16x32_bf16 v[72:75], v[100:103], a[16:19], v[72:75]\n"
        "v_mfma_f32_16x16x32_bf16 v[76:79], v[96:99], a[4:7], 0\n"
        "v_mfma_f32_16x16x32_bf16 v[76:79], v[100:103], a[20:23], v[76:79]\n"
        "v_mfma_f32_16x16x32_bf16 v[80:83], v[96:99], a[8:11], 0\n"
        "ds_write_b128 v47, v[60:63] offset:0\n"
        "ds_write_b128 v47, v[64:67] offset:1024\n"
        "v_mfma_f32_16x16x32_bf16 v[80:83], v[100:103], a[24:27], v[80:83]\n"
        "ds_write_b128 v47, v[68:71] offset:2048\n"
        "v_mfma_f32_16x16x32_bf16 v[84:87], v[96:99], a[12:15], 0\n"
        "v_mfma_f32_16x16x32_bf16 v[84:87], v[100:103], a[28:31], v[84:87]\n"
        "s_waitcnt vmcnt(12)\n"
        "v_mfma_f32_16x16x32_bf16 v[72:75], v[104:107], a[32:35], v[72:75]\n"
        "global_load_dwordx4 v[96:99], v192, s[78:79] offset:0 sc1\n"
        "global_load_dwordx4 v[100:103], v192, s[78:79] offset:1024 sc1\n"
        "v_mfma_f32_16x16x32_bf16 v[72:75], v[108:111], a[48:51], v[72:75]\n"
        "v_mfma_f32_16x16x32_bf16 v[76:79], v[104:107], a[36:39], v[76:79]\n"
        "v_mfma_f32_16x16x32_bf16 v[76:79], v[108:111], a[52:55], v[76:79]\n"
        "v_mfma_f32_16x16x32_bf16 v[80:83], v[104:107], a[40:43], v[80:83]\n"
        "s_waitcnt lgkmcnt(0)\n"
        "s_barrier\n"
        "v_mfma_f32_16x16x32_bf16 v[80:83], v[108:111], a[56:59], v[80:83]\n"
        "ds_read_b128 v[4:7], v48 offset:0\n"
        "ds_read_b128 v[8:11], v49 offset:0\n"
        "v_mfma_f32_16x16x32_bf16 v[84:87], v[104:107], a[44:47], v[84:87]\n"
        "ds_read_b128 v[12:15], v50 offset:0\n"
        "v_mfma_f32_16x16x32_bf16 v[84:87], v[108:111], a[60:63], v[84:87]\n"
        "s_waitcnt vmcnt(12)\n"
        "v_mfma_f32_16x16x32_bf16 v[72:75], v[112:115], a[64:67], v[72:75]\n"
        "global_load_dwordx4 v[104:107], v193, s[78:79] offset:0 sc1\n"
        "global_load_dwordx4 v[108:111], v193, s[78:79] offset:1024 sc1\n"
        "v_mfma_f32_16x16x32_bf16 v[72:75], v[116:119], a[80:83], v[72:75]\n"
        "v_mfma_f32_16x16x32_bf16 v[76:79], v[112:115], a[68:71], v[76:79]\n"
        "v_mfma_f32_16x16x32_bf16 v[76:79], v[116:119], a[84:87], v[76:79]\n"
        "v_mfma_f32_16x16x32_bf16 v[80:83], v[112:115], a[72:75], v[80:83]\n"
        "s_waitcnt lgkmcnt(0)\n"
        "v_add_f32 v4, v4, v8\n"
        "v_mfma_f32_16x16x32_bf16 v[80:83], v[116:119], a[88:91], v[80:83]\n"
        "v_add_f32 v5, v5, v9\n"
        "v_add_f32 v6, v6, v10\n"
        "v_mfma_f32_16x16x32_bf16 v[84:87], v[112:115], a[76:79], v[84:87]\n"
        "v_add_f32 v7, v7, v11\n"
        "v_add_f32 v12, v56, v12\n"
        "v_mfma_f32_16x16x32_bf16 v[84:87], v[116:119], a[92:95], v[84:87]\n"
        "v_add_f32 v13, v57, v13\n"
        "s_waitcnt vmcnt(12)\n"
        "v_mfma_f32_16x16x32_bf16 v[72:75], v[120:123], a[96:99], v[72:75]\n"
        "global_load_dwordx4 v[112:115], v194, s[78:79] offset:0 sc1\n"
        "global_load_dwordx4 v[116:119], v194, s[78:79] offset:1024 sc1\n"
        "v_mfma_f32_16x16x32_bf16 v[72:75], v[124:127], a[112:115], v[72:75]\n"
        "v_add_f32 v14, v58, v14\n"
        "v_add_f32 v15, v59, v15\n"
        "v_mfma_f32_16x16x32_bf16 v[76:79], v[120:123], a[100:103], v[76:79]\n"
        "v_add_f32 v12, v12, v4\n"
        "v_add_f32 v13, v13, v5\n"
        "v_mfma_f32_16x16x32_bf16 v[76:79], v[124:127], a[116:119], v[76:79]\n"
        "v_add_f32 v14, v14, v6\n"
        "v_add_f32 v15, v15, v7\n"
        "v_mfma_f32_16x16x32_bf16 v[80:83], v[120:123], a[104:107], v[80:83]\n"
        "v_cvt_pk_bf16_f32 v232, v12, v13\n"
        "v_cvt_pk_bf16_f32 v233, v14, v15\n"
        "v_mfma_f32_16x16x32_bf16 v[80:83], v[124:127], a[120:123], v[80:83]\n"
        "v_min_u32 v232, -2, v232\n"
        "v_min_u32 v233, -2, v233\n"
        "v_mfma_f32_16x16x32_bf16 v[84:87], v[120:123], a[108:111], v[84:87]\n"
        "v_mfma_f32_16x16x32_bf16 v[84:87], v[124:127], a[124:127], v[84:87]\n"
        "s_waitcnt vmcnt(12)\n"
        "v_mfma_f32_16x16x32_bf16 v[72:75], v[128:131], a[128:131], v[72:75]\n"
        "global_load_dwordx4 v[120:123], v195, s[78:79] offset:0 sc1\n"
        "global_load_dwordx4 v[124:127], v195, s[78:79] offset:1024 sc1\n"
        "v_mfma_f32_16x16x32_bf16 v[72:75], v[132:135], a[144:147], v[72:75]\n"
        "v_mfma_f32_16x16x32_bf16 v[76:79], v[128:131], a[132:135], v[76:79]\n"
        "v_mfma_f32_16x16x32_bf16 v[76:79], v[132:135], a[148:151], v[76:79]\n"
        "v_mfma_f32_16x16x32_bf16 v[80:83], v[128:131], a[136:139], v[80:83]\n"
        "v_mfma_f32_16x16x32_bf16 v[80:83], v[132:135], a[152:155], v[80:83]\n"
        "v_mfma_f32_16x16x32_bf16 v[84:87], v[128:131], a[140:143], v[84:87]\n"
        "v_mfma_f32_16x16x32_bf16 v[84:87], v[132:135], a[156:159], v[84:87]\n"
        "s_waitcnt vmcnt(12)\n"
        "v_mfma_f32_16x16x32_bf16 v[72:75], v[136:139], a[160:163], v[72:75]\n"
        "global_load_dwordx4 v[128:131], v196, s[78:79] offset:0 sc1\n"
        "global_load_dwordx4 v[132:135], v196, s[78:79] offset:1024 sc1\n"
        "v_mfma_f32_16x16x32_bf16 v[72:75], v[140:143], a[176:179], v[72:75]\n"
        "v_mfma_f32_16x16x32_bf16 v[76:79], v[136:139], a[164:167], v[76:79]\n"
        "v_mfma_f32_16x16x32_bf16 v[76:79], v[140:143], a[180:183], v[76:79]\n"
        "v_mfma_f32_16x16x32_bf16 v[80:83], v[136:139], a[168:171], v[80:83]\n"
        "v_mfma_f32_16x16x32_bf16 v[80:83], v[140:143], a[184:187], v[80:83]\n"
        "v_mfma_f32_16x16x32_bf16 v[84:87], v[136:139], a[172:175], v[84:87]\n"
        "v_mfma_f32_16x16x32_bf16 v[84:87], v[140:143], a[188:191], v[84:87]\n"
        "s_waitcnt vmcnt(12)\n"
        "v_mfma_f32_16x16x32_bf16 v[72:75], v[144:147], a[192:195], v[72:75]\n"
        "global_load_dwordx4 v[136:139], v197, s[78:79] offset:0 sc1\n"
        "global_load_dwordx4 v[140:143], v197, s[78:79] offset:1024 sc1\n"
        "v_mfma_f32_16x16x32_bf16 v[72:75], v[148:151], a[208:211], v[72:75]\n"
        "v_mfma_f32_16x16x32_bf16 v[76:79], v[144:147], a[196:199], v[76:79]\n"
        "v_mfma_f32_16x16x32_bf16 v[76:79], v[148:151], a[212:215], v[76:79]\n"
        "v_mfma_f32_16x16x32_bf16 v[80:83], v[144:147], a[200:203], v[80:83]\n"
        "v_mfma_f32_16x16x32_bf16 v[80:83], v[148:151], a[216:219], v[80:83]\n"
        "v_mfma_f32_16x16x32_bf16 v[84:87], v[144:147], a[204:207], v[84:87]\n"
        "v_mfma_f32_16x16x32_bf16 v[84:87], v[148:151], a[220:223], v[84:87]\n"
        "s_waitcnt vmcnt(12)\n"
        "v_mfma_f32_16x16x32_bf16 v[72:75], v[152:155], a[224:227], v[72:75]\n"
        "global_load_dwordx4 v[144:147], v198, s[78:79] offset:0 sc1\n"
        "global_load_dwordx4 v[148:151], v198, s[78:79] offset:1024 sc1\n"
        "v_mfma_f32_16x16x32_bf16 v[72:75], v[156:159], a[240:243], v[72:75]\n"
        "v_mfma_f32_16x16x32_bf16 v[76:79], v[152:155], a[228:231], v[76:79]\n"
        "v_mfma_f32_16x16x32_bf16 v[76:79], v[156:159], a[244:247], v[76:79]\n"
        "v_mfma_f32_16x16x32_bf16 v[80:83], v[152:155], a[232:235], v[80:83]\n"
        "v_mfma_f32_16x16x32_bf16 v[80:83], v[156:159], a[248:251], v[80:83]\n"
        "v_mfma_f32_16x16x32_bf16 v[84:87], v[152:155], a[236:239], v[84:87]\n"
        "v_mfma_f32_16x16x32_bf16 v[84:87], v[156:159], a[252:255], v[84:87]\n"
        "s_waitcnt vmcnt(12)\n"
        "v_mfma_f32_16x16x32_bf16 v[56:59], v[96:99], a[0:3], 0\n"
        "global_load_dwordx4 v[152:155], v199, s[78:79] offset:0 sc1\n"
        "global_load_dwordx4 v[156:159], v199, s[78:79] offset:1024 sc1\n"
        "s_add_u32 s78, s78, 0x10080\n"
        "s_addc_u32 s79, s79, 0\n"
        "v_mfma_f32_16x16x32_bf16 v[56:59], v[100:103], a[16:19], v[56:59]\n"
        "v_mfma_f32_16x16x32_bf16 v[60:63], v[96:99], a[4:7], 0\n"
        "v_mfma_f32_16x16x32_bf16 v[60:63], v[100:103], a[20:23], v[60:63]\n"
        "v_mfma_f32_16x16x32_bf16 v[64:67], v[96:99], a[8:11], 0\n"
        "ds_write_b128 v47, v[76:79] offset:3072\n"
        "ds_write_b128 v47, v[80:83] offset:4096\n"
        "v_mfma_f32_16x16x32_bf16 v[64:67], v[100:103], a[24:27], v[64:67]\n"
        "ds_write_b128 v47, v[84:87] offset:5120\n"
        "v_mfma_f32_16x16x32_bf16 v[68:71], v[96:99], a[12:15], 0\n"
        "v_mfma_f32_16x16x32_bf16 v[68:71], v[100:103], a[28:31], v[68:71]\n"
        "s_waitcnt vmcnt(12)\n"
        "v_mfma_f32_16x16x32_bf16 v[56:59], v[104:107], a[32:35], v[56:59]\n"
        "global_load_dwordx4 v[96:99], v192, s[78:79] offset:0 sc1\n"
        "global_load_dwordx4 v[100:103], v192, s[78:79] offset:1024 sc1\n"
        "v_mfma_f32_16x16x32_bf16 v[56:59], v[108:111], a[48:51], v[56:59]\n"
        "v_mfma_f32_16x16x32_bf16 v[60:63], v[104:107], a[36:39], v[60:63]\n"
        "v_mfma_f32_16x16x32_bf16 v[60:63], v[108:111], a[52:55], v[60:63]\n"
        "v_mfma_f32_16x16x32_bf16 v[64:67], v[104:107], a[40:43], v[64:67]\n"
        "s_waitcnt lgkmcnt(0)\n"
        "s_barrier\n"
        "v_mfma_f32_16x16x32_bf16 v[64:67], v[108:111], a[56:59], v[64:67]\n"
        "ds_read_b128 v[4:7], v48 offset:3072\n"
        "ds_read_b128 v[8:11], v49 offset:3072\n"
        "v_mfma_f32_16x16x32_bf16 v[68:71], v[104:107], a[44:47], v[68:71]\n"
        "ds_read_b128 v[12:15], v50 offset:3072\n"
        "v_mfma_f32_16x16x32_bf16 v[68:71], v[108:111], a[60:63], v[68:71]\n"
        "s_waitcnt vmcnt(12)\n"
        "v_mfma_f32_16x16x32_bf16 v[56:59], v[112:115], a[64:67], v[56:59]\n"
        "global_load_dwordx4 v[104:107], v193, s[78:79] offset:0 sc1\n"
        "global_load_dwordx4 v[108:111], v193, s[78:79] offset:1024 sc1\n"
        "v_mfma_f32_16x16x32_bf16 v[56:59], v[116:119], a[80:83], v[56:59]\n"
        "v_mfma_f32_16x16x32_bf16 v[60:63], v[112:115], a[68:71], v[60:63]\n"
        "v_mfma_f32_16x16x32_bf16 v[60:63], v[116:119], a[84:87], v[60:63]\n"
        "v_mfma_f32_16x16x32_bf16 v[64:67], v[112:115], a[72:75], v[64:67]\n"
        "s_waitcnt lgkmcnt(0)\n"
        "v_add_f32 v4, v4, v8\n"
        "v_mfma_f32_16x16x32_bf16 v[64:67], v[116:119], a[88:91], v[64:67]\n"
        "v_add_f32 v5, v5, v9\n"
        "v_add_f32 v6, v6, v10\n"
        "v_mfma_f32_16x16x32_bf16 v[68:71], v[112:115], a[76:79], v[68:71]\n"
        "v_add_f32 v7, v7, v11\n"
        "v_add_f32 v12, v72, v12\n"
        "v_mfma_f32_16x16x32_bf16 v[68:71], v[116:119], a[92:95], v[68:71]\n"
        "v_add_f32 v13, v73, v13\n"
        "s_waitcnt vmcnt(12)\n"
        "v_mfma_f32_16x16x32_bf16 v[56:59], v[120:123], a[96:99], v[56:59]\n"
        "global_load_dwordx4 v[112:115], v194, s[78:79] offset:0 sc1\n"
        "global_load_dwordx4 v[116:119], v194, s[78:79] offset:1024 sc1\n"
        "v_mfma_f32_16x16x32_bf16 v[56:59], v[124:127], a[112:115], v[56:59]\n"
        "v_add_f32 v14, v74, v14\n"
        "v_add_f32 v15, v75, v15\n"
        "v_mfma_f32_16x16x32_bf16 v[60:63], v[120:123], a[100:103], v[60:63]\n"
        "v_add_f32 v12, v12, v4\n"
        "v_add_f32 v13, v13, v5\n"
        "v_mfma_f32_16x16x32_bf16 v[60:63], v[124:127], a[116:119], v[60:63]\n"
        "v_add_f32 v14, v14, v6\n"
        "v_add_f32 v15, v15, v7\n"
        "v_mfma_f32_16x16x32_bf16 v[64:67], v[120:123], a[104:107], v[64:67]\n"
        "v_cvt_pk_bf16_f32 v234, v12, v13\n"
        "v_cvt_pk_bf16_f32 v235, v14, v15\n"
        "v_mfma_f32_16x16x32_bf16 v[64:67], v[124:127], a[120:123], v[64:67]\n"
        "v_min_u32 v234, -2, v234\n"
        "v_min_u32 v235, -2, v235\n"
        "v_mfma_f32_16x16x32_bf16 v[68:71], v[120:123], a[108:111], v[68:71]\n"
        "v_mfma_f32_16x16x32_bf16 v[68:71], v[124:127], a[124:127], v[68:71]\n"
        "s_waitcnt vmcnt(12)\n"
        "v_mfma_f32_16x16x32_bf16 v[56:59], v[128:131], a[128:131], v[56:59]\n"
        "global_load_dwordx4 v[120:123], v195, s[78:79] offset:0 sc1\n"
        "global_load_dwordx4 v[124:127], v195, s[78:79] offset:1024 sc1\n"
        "v_mfma_f32_16x16x32_bf16 v[56:59], v[132:135], a[144:147], v[56:59]\n"
        "v_mfma_f32_16x16x32_bf16 v[60:63], v[128:131], a[132:135], v[60:63]\n"
        "v_mfma_f32_16x16x32_bf16 v[60:63], v[132:135], a[148:151], v[60:63]\n"
        "v_mfma_f32_16x16x32_bf16 v[64:67], v[128:131], a[136:139], v[64:67]\n"
        "v_mfma_f32_16x16x32_bf16 v[64:67], v[132:135], a[152:155], v[64:67]\n"
        "v_mfma_f32_16x16x32_bf16 v[68:71], v[128:131], a[140:143], v[68:71]\n"
        "v_mfma_f32_16x16x32_bf16 v[68:71], v[132:135], a[156:159], v[68:71]\n"
        "s_waitcnt vmcnt(12)\n"
        "v_mfma_f32_16x16x32_bf16 v[56:59], v[136:139], a[160:163], v[56:59]\n"
        "global_load_dwordx4 v[128:131], v196, s[78:79] offset:0 sc1\n"
        "global_load_dwordx4 v[132:135], v196, s[78:79] offset:1024 sc1\n"
        "v_mfma_f32_16x16x32_bf16 v[56:59], v[140:143], a[176:179], v[56:59]\n"
        "v_mfma_f32_16x16x32_bf16 v[60:63], v[136:139], a[164:167], v[60:63]\n"
        "v_mfma_f32_16x16x32_bf16 v[60:63], v[140:143], a[180:183], v[60:63]\n"
        "v_mfma_f32_16x16x32_bf16 v[64:67], v[136:139], a[168:171], v[64:67]\n"
        "v_mfma_f32_16x16x32_bf16 v[64:67], v[140:143], a[184:187], v[64:67]\n"
        "v_mfma_f32_16x16x32_bf16 v[68:71], v[136:139], a[172:175], v[68:71]\n"
        "v_mfma_f32_16x16x32_bf16 v[68:71], v[140:143], a[188:191], v[68:71]\n"
        "s_waitcnt vmcnt(12)\n"
        "v_mfma_f32_16x16x32_bf16 v[56:59], v[144:147], a[192:195], v[56:59]\n"
        "global_load_dwordx4 v[136:139], v197, s[78:79] offset:0 sc1\n"
        "global_load_dwordx4 v[140:143], v197, s[78:79] offset:1024 sc1\n"
        "v_mfma_f32_16x16x32_bf16 v[56:59], v[148:151], a[208:211], v[56:59]\n"
        "v_mfma_f32_16x16x32_bf16 v[60:63], v[144:147], a[196:199], v[60:63]\n"
        "v_mfma_f32_16x16x32_bf16 v[60:63], v[148:151], a[212:215], v[60:63]\n"
        "v_mfma_f32_16x16x32_bf16 v[64:67], v[144:147], a[200:203], v[64:67]\n"
        "v_mfma_f32_16x16x32_bf16 v[64:67], v[148:151], a[216:219], v[64:67]\n"
        "v_mfma_f32_16x16x32_bf16 v[68:71], v[144:147], a[204:207], v[68:71]\n"
        "v_mfma_f32_16x16x32_bf16 v[68:71], v[148:151], a[220:223], v[68:71]\n"
        "s_waitcnt vmcnt(12)\n"
        "v_mfma_f32_16x16x32_bf16 v[56:59], v[152:155], a[224:227], v[56:59]\n"
        "global_load_dwordx4 v[144:147], v198, s[78:79] offset:0 sc1\n"
        "global_load_dwordx4 v[148:151], v198, s[78:79] offset:1024 sc1\n"
        "v_mfma_f32_16x16x32_bf16 v[56:59], v[156:159], a[240:243], v[56:59]\n"
        "v_mfma_f32_16x16x32_bf16 v[60:63], v[152:155], a[228:231], v[60:63]\n"
        "v_mfma_f32_16x16x32_bf16 v[60:63], v[156:159], a[244:247], v[60:63]\n"
        "v_mfma_f32_16x16x32_bf16 v[64:67], v[152:155], a[232:235], v[64:67]\n"
        "v_mfma_f32_16x16x32_bf16 v[64:67], v[156:159], a[248:251], v[64:67]\n"
        "v_mfma_f32_16x16x32_bf16 v[68:71], v[152:155], a[236:239], v[68:71]\n"
        "v_mfma_f32_16x16x32_bf16 v[68:71], v[156:159], a[252:255], v[68:71]\n"
        "s_waitcnt vmcnt(12)\n"
        "v_mfma_f32_16x16x32_bf16 v[72:75], v[96:99], a[0:3], 0\n"
        "global_load_dwordx4 v[152:155], v199, s[78:79] offset:0 sc1\n"
        "global_load_dwordx4 v[156:159], v199, s[78:79] offset:1024 sc1\n"
        "s_add_u32 s78, s78, 0x10080\n"
        "s_addc_u32 s79, s79, 0\n"
        "v_mfma_f32_16x16x32_bf16 v[72:75], v[100:103], a[16:19], v[72:75]\n"
        "v_mfma_f32_16x16x32_bf16 v[76:79], v[96:99], a[4:7], 0\n"
        "v_mfma_f32_16x16x32_bf16 v[76:79], v[100:103], a[20:23], v[76:79]\n"
        "v_mfma_f32_16x16x32_bf16 v[80:83], v[96:99], a[8:11], 0\n"
        "ds_write_b128 v47, v[60:63] offset:0\n"
        "ds_write_b128 v47, v[64:67] offset:1024\n"
        "v_mfma_f32_16x16x32_bf16 v[80:83], v[100:103], a[24:27], v[80:83]\n"
        "ds_write_b128 v47, v[68:71] offset:2048\n"
        "v_mfma_f32_16x16x32_bf16 v[84:87], v[96:99], a[12:15], 0\n"
        "v_mfma_f32_16x16x32_bf16 v[84:87], v[100:103], a[28:31], v[84:87]\n"
        "s_waitcnt vmcnt(12)\n"
        "v_mfma_f32_16x16x32_bf16 v[72:75], v[104:107], a[32:35], v[72:75]\n"
        "global_load_dwordx4 v[96:99], v192, s[78:79] offset:0 sc1\n"
        "global_load_dwordx4 v[100:103], v192, s[78:79] offset:1024 sc1\n"
        "v_mfma_f32_16x16x32_bf16 v[72:75], v[108:111], a[48:51], v[72:75]\n"
        "v_mfma_f32_16x16x32_bf16 v[76:79], v[104:107], a[36:39], v[76:79]\n"
        "v_mfma_f32_16x16x32_bf16 v[76:79], v[108:111], a[52:55], v[76:79]\n"
        "v_mfma_f32_16x16x32_bf16 v[80:83], v[104:107], a[40:43], v[80:83]\n"
        "s_waitcnt lgkmcnt(0)\n"
        "s_barrier\n"
        "v_mfma_f32_16x16x32_bf16 v[80:83], v[108:111], a[56:59], v[80:83]\n"
        "ds_read_b128 v[4:7], v48 offset:0\n"
        "ds_read_b128 v[8:11], v49 offset:0\n"
        "v_mfma_f32_16x16x32_bf16 v[84:87], v[104:107], a[44:47], v[84:87]\n"
        "ds_read_b128 v[12:15], v50 offset:0\n"
        "v_mfma_f32_16x16x32_bf16 v[84:87], v[108:111], a[60:63], v[84:87]\n"
        "s_waitcnt vmcnt(12)\n"
        "v_mfma_f32_16x16x32_bf16 v[72:75], v[112:115], a[64:67], v[72:75]\n"
        "global_load_dwordx4 v[104:107], v193, s[78:79] offset:0 sc1\n"
        "global_load_dwordx4 v[108:111], v193, s[78:79] offset:1024 sc1\n"
        "v_mfma_f32_16x16x32_bf16 v[72:75], v[116:119], a[80:83], v[72:75]\n"
        "v_mfma_f32_16x16x32_bf16 v[76:79], v[112:115], a[68:71], v[76:79]\n"
        "v_mfma_f32_16x16x32_bf16 v[76:79], v[116:119], a[84:87], v[76:79]\n"
        "v_mfma_f32_16x16x32_bf16 v[80:83], v[112:115], a[72:75], v[80:83]\n"
        "s_waitcnt lgkmcnt(0)\n"
        "v_add_f32 v4, v4, v8\n"
        "v_mfma_f32_16x16x32_bf16 v[80:83], v[116:119], a[88:91], v[80:83]\n"
        "v_add_f32 v5, v5, v9\n"
        "v_add_f32 v6, v6, v10\n"
        "v_mfma_f32_16x16x32_bf16 v[84:87], v[112:115], a[76:79], v[84:87]\n"
        "v_add_f32 v7, v7, v11\n"
        "v_add_f32 v12, v56, v12\n"
        "v_mfma_f32_16x16x32_bf16 v[84:87], v[116:119], a[92:95], v[84:87]\n"
        "v_add_f32 v13, v57, v13\n"
        "s_waitcnt vmcnt(12)\n"
        "v_mfma_f32_16x16x32_bf16 v[72:75], v[120:123], a[96:99], v[72:75]\n"
        "global_load_dwordx4 v[112:115], v194, s[78:79] offset:0 sc1\n"
        "global_load_dwordx4 v[116:119], v194, s[78:79] offset:1024 sc1\n"
        "v_mfma_f32_16x16x32_bf16 v[72:75], v[124:127], a[112:115], v[72:75]\n"
        "v_add_f32 v14, v58, v14\n"
        "v_add_f32 v15, v59, v15\n"
        "v_mfma_f32_16x16x32_bf16 v[76:79], v[120:123], a[100:103], v[76:79]\n"
        "v_add_f32 v12, v12, v4\n"
        "v_add_f32 v13, v13, v5\n"
        "v_mfma_f32_16x16x32_bf16 v[76:79], v[124:127], a[116:119], v[76:79]\n"
        "v_add_f32 v14, v14, v6\n"
        "v_add_f32 v15, v15, v7\n"
        "v_mfma_f32_16x16x32_bf16 v[80:83], v[120:123], a[104:107], v[80:83]\n"
        "v_cvt_pk_bf16_f32 v236, v12, v13\n"
        "v_cvt_pk_bf16_f32 v237, v14, v15\n"
        "v_mfma_f32_16x16x32_bf16 v[80:83], v[124:127], a[120:123], v[80:83]\n"
        "v_min_u32 v236, -2, v236\n"
        "v_min_u32 v237, -2, v237\n"
        "v_mfma_f32_16x16x32_bf16 v[84:87], v[120:123], a[108:111], v[84:87]\n"
        "v_mfma_f32_16x16x32_bf16 v[84:87], v[124:127], a[124:127], v[84:87]\n"
        "s_waitcnt vmcnt(12)\n"
        "v_mfma_f32_16x16x32_bf16 v[72:75], v[128:131], a[128:131], v[72:75]\n"
        "global_load_dwordx4 v[120:123], v195, s[78:79] offset:0 sc1\n"
        "global_load_dwordx4 v[124:127], v195, s[78:79] offset:1024 sc1\n"
        "v_mfma_f32_16x16x32_bf16 v[72:75], v[132:135], a[144:147], v[72:75]\n"
        "global_store_dwordx4 v44, v[228:231], s[40:41] offset:0 sc1\n"
        "global_store_dwordx4 v44, v[232:235], s[40:41] offset:1024 sc1\n"
        "v_mfma_f32_16x16x32_bf16 v[76:79], v[128:131], a[132:135], v[76:79]\n"
        "global_store_dwordx2 v44, v[236:237], s[40:41] offset:2048 sc1\n"
        "v_mfma_f32_16x16x32_bf16 v[76:79], v[132:135], a[148:151], v[76:79]\n"
        "v_mfma_f32_16x16x32_bf16 v[80:83], v[128:131], a[136:139], v[80:83]\n"
        "v_mfma_f32_16x16x32_bf16 v[80:83], v[132:135], a[152:155], v[80:83]\n"
        "v_mfma_f32_16x16x32_bf16 v[84:87], v[128:131], a[140:143], v[84:87]\n"
        "v_mfma_f32_16x16x32_bf16 v[84:87], v[132:135], a[156:159], v[84:87]\n"
        "s_waitcnt vmcnt(15)\n"
        "v_mfma_f32_16x16x32_bf16 v[72:75], v[136:139], a[160:163], v[72:75]\n"
        "global_load_dwordx4 v[128:131], v196, s[78:79] offset:0 sc1\n"
        "global_load_dwordx4 v[132:135], v196, s[78:79] offset:1024 sc1\n"
        "v_mfma_f32_16x16x32_bf16 v[72:75], v[140:143], a[176:179], v[72:75]\n"
        "v_mfma_f32_16x16x32_bf16 v[76:79], v[136:139], a[164:167], v[76:79]\n"
        "v_mfma_f32_16x16x32_bf16 v[76:79], v[140:143], a[180:183], v[76:79]\n"
        "v_mfma_f32_16x16x32_bf16 v[80:83], v[136:139], a[168:171], v[80:83]\n"
        "v_mfma_f32_16x16x32_bf16 v[80:83], v[140:143], a[184:187], v[80:83]\n"
        "v_mfma_f32_16x16x32_bf16 v[84:87], v[136:139], a[172:175], v[84:87]\n"
        "v_mfma_f32_16x16x32_bf16 v[84:87], v[140:143], a[188:191], v[84:87]\n"
        "s_waitcnt vmcnt(15)\n"
        "v_mfma_f32_16x16x32_bf16 v[72:75], v[144:147], a[192:195], v[72:75]\n"
        "global_load_dwordx4 v[136:139], v197, s[78:79] offset:0 sc1\n"
        "global_load_dwordx4 v[140:143], v197, s[78:79] offset:1024 sc1\n"
        "v_mfma_f32_16x16x32_bf16 v[72:75], v[148:151], a[208:211], v[72:75]\n"
        "v_mfma_f32_16x16x32_bf16 v[76:79], v[144:147], a[196:199], v[76:79]\n"
        "v_mfma_f32_16x16x32_bf16 v[76:79], v[148:151], a[212:215], v[76:79]\n"
        "v_mfma_f32_16x16x32_bf16 v[80:83], v[144:147], a[200:203], v[80:83]\n"
        "v_mfma_f32_16x16x32_bf16 v[80:83], v[148:151], a[216:219], v[80:83]\n"
        "v_mfma_f32_16x16x32_bf16 v[84:87], v[144:147], a[204:207], v[84:87]\n"
        "v_mfma_f32_16x16x32_bf16 v[84:87], v[148:151], a[220:223], v[84:87]\n"
        "s_waitcnt vmcnt(15)\n"
        "v_mfma_f32_16x16x32_bf16 v[72:75], v[152:155], a[224:227], v[72:75]\n"
        "global_load_dwordx4 v[144:147], v198, s[78:79] offset:0 sc1\n"
        "global_load_dwordx4 v[148:151], v198, s[78:79] offset:1024 sc1\n"
        "v_mfma_f32_16x16x32_bf16 v[72:75], v[156:159], a[240:243], v[72:75]\n"
        "v_mfma_f32_16x16x32_bf16 v[76:79], v[152:155], a[228:231], v[76:79]\n"
        "v_mfma_f32_16x16x32_bf16 v[76:79], v[156:159], a[244:247], v[76:79]\n"
        "v_mfma_f32_16x16x32_bf16 v[80:83], v[152:155], a[232:235], v[80:83]\n"
        "v_mfma_f32_16x16x32_bf16 v[80:83], v[156:159], a[248:251], v[80:83]\n"
        "v_mfma_f32_16x16x32_bf16 v[84:87], v[152:155], a[236:239], v[84:87]\n"
        "v_mfma_f32_16x16x32_bf16 v[84:87], v[156:159], a[252:255], v[84:87]\n"
        "s_waitcnt vmcnt(15)\n"
        "v_mfma_f32_16x16x32_bf16 v[56:59], v[96:99], a[0:3], 0\n"
        "global_load_dwordx4 v[152:155], v199, s[78:79] offset:0 sc1\n"
        "global_load_dwordx4 v[156:159], v199, s[78:79] offset:1024 sc1\n"
        "s_add_u32 s78, s78, 0x10080\n"
        "s_addc_u32 s79, s79, 0\n"
        "v_mfma_f32_16x16x32_bf16 v[56:59], v[100:103], a[16:19], v[56:59]\n"
        "v_mfma_f32_16x16x32_bf16 v[60:63], v[96:99], a[4:7], 0\n"
        "v_mfma_f32_16x16x32_bf16 v[60:63], v[100:103], a[20:23], v[60:63]\n"
        "v_mfma_f32_16x16x32_bf16 v[64:67], v[96:99], a[8:11], 0\n"
        "ds_write_b128 v47, v[76:79] offset:3072\n"
        "ds_write_b128 v47, v[80:83] offset:4096\n"
        "v_mfma_f32_16x16x32_bf16 v[64:67], v[100:103], a[24:27], v[64:67]\n"
        "ds_write_b128 v47, v[84:87] offset:5120\n"
        "v_mfma_f32_16x16x32_bf16 v[68:71], v[96:99], a[12:15], 0\n"
        "v_mfma_f32_16x16x32_bf16 v[68:71], v[100:103], a[28:31], v[68:71]\n"
        "s_waitcnt vmcnt(15)\n"
        "v_mfma_f32_16x16x32_bf16 v[56:59], v[104:107], a[32:35], v[56:59]\n"
        "global_load_dwordx4 v[96:99], v192, s[78:79] offset:0 sc1\n"
        "global_load_dwordx4 v[100:103], v192, s[78:79] offset:1024 sc1\n"
        "v_mfma_f32_16x16x32_bf16 v[56:59], v[108:111], a[48:51], v[56:59]\n"
        "v_mfma_f32_16x16x32_bf16 v[60:63], v[104:107], a[36:39], v[60:63]\n"
        "v_mfma_f32_16x16x32_bf16 v[60:63], v[108:111], a[52:55], v[60:63]\n"
        "v_mfma_f32_16x16x32_bf16 v[64:67], v[104:107], a[40:43], v[64:67]\n"
        "s_waitcnt lgkmcnt(0)\n"
        "s_barrier\n"
        "v_mfma_f32_16x16x32_bf16 v[64:67], v[108:111], a[56:59], v[64:67]\n"
        "ds_read_b128 v[4:7], v48 offset:3072\n"
        "ds_read_b128 v[8:11], v49 offset:3072\n"
        "v_mfma_f32_16x16x32_bf16 v[68:71], v[104:107], a[44:47], v[68:71]\n"
        "ds_read_b128 v[12:15], v50 offset:3072\n"
        "v_mfma_f32_16x16x32_bf16 v[68:71], v[108:111], a[60:63], v[68:71]\n"
        "s_waitcnt vmcnt(15)\n"
        "v_mfma_f32_16x16x32_bf16 v[56:59], v[112:115], a[64:67], v[56:59]\n"
        "global_load_dwordx4 v[104:107], v193, s[78:79] offset:0 sc1\n"
        "global_load_dwordx4 v[108:111], v193, s[78:79] offset:1024 sc1\n"
        "v_mfma_f32_16x16x32_bf16 v[56:59], v[116:119], a[80:83], v[56:59]\n"
        "v_mfma_f32_16x16x32_bf16 v[60:63], v[112:115], a[68:71], v[60:63]\n"
        "v_mfma_f32_16x16x32_bf16 v[60:63], v[116:119], a[84:87], v[60:63]\n"
        "v_mfma_f32_16x16x32_bf16 v[64:67], v[112:115], a[72:75], v[64:67]\n"
        "s_waitcnt lgkmcnt(0)\n"
        "v_add_f32 v4, v4, v8\n"
        "v_mfma_f32_16x16x32_bf16 v[64:67], v[116:119], a[88:91], v[64:67]\n"
        "v_add_f32 v5, v5, v9\n"
        "v_add_f32 v6, v6, v10\n"
        "v_mfma_f32_16x16x32_bf16 v[68:71], v[112:115], a[76:79], v[68:71]\n"
        "v_add_f32 v7, v7, v11\n"
        "v_add_f32 v208, v72, v12\n"
        "v_mfma_f32_16x16x32_bf16 v[68:71], v[116:119], a[92:95], v[68:71]\n"
        "v_add_f32 v209, v73, v13\n"
        "s_waitcnt vmcnt(15)\n"
        "v_mfma_f32_16x16x32_bf16 v[56:59], v[120:123], a[96:99], v[56:59]\n"
        "global_load_dwordx4 v[112:115], v194, s[78:79] offset:0 sc1\n"
        "global_load_dwordx4 v[116:119], v194, s[78:79] offset:1024 sc1\n"
        "v_mfma_f32_16x16x32_bf16 v[56:59], v[124:127], a[112:115], v[56:59]\n"
        "v_add_f32 v210, v74, v14\n"
        "v_add_f32 v211, v75, v15\n"
        "v_mfma_f32_16x16x32_bf16 v[60:63], v[120:123], a[100:103], v[60:63]\n"
        "v_add_f32 v208, v208, v4\n"
        "v_add_f32 v209, v209, v5\n"
        "v_mfma_f32_16x16x32_bf16 v[60:63], v[124:127], a[116:119], v[60:63]\n"
        "v_add_f32 v210, v210, v6\n"
        "v_add_f32 v211, v211, v7\n"
        "v_mfma_f32_16x16x32_bf16 v[64:67], v[120:123], a[104:107], v[64:67]\n"
        "v_mfma_f32_16x16x32_bf16 v[64:67], v[124:127], a[120:123], v[64:67]\n"
        "v_mfma_f32_16x16x32_bf16 v[68:71], v[120:123], a[108:111], v[68:71]\n"
        "v_mfma_f32_16x16x32_bf16 v[68:71], v[124:127], a[124:127], v[68:71]\n"
        "s_waitcnt vmcnt(12)\n"
        "v_mfma_f32_16x16x32_bf16 v[56:59], v[128:131], a[128:131], v[56:59]\n"
        "global_load_dwordx4 v[120:123], v195, s[78:79] offset:0 sc1\n"
        "global_load_dwordx4 v[124:127], v195, s[78:79] offset:1024 sc1\n"
        "v_mfma_f32_16x16x32_bf16 v[56:59], v[132:135], a[144:147], v[56:59]\n"
        "v_mfma_f32_16x16x32_bf16 v[60:63], v[128:131], a[132:135], v[60:63]\n"
        "v_mfma_f32_16x16x32_bf16 v[60:63], v[132:135], a[148:151], v[60:63]\n"
        "v_mfma_f32_16x16x32_bf16 v[64:67], v[128:131], a[136:139], v[64:67]\n"
        "v_mfma_f32_16x16x32_bf16 v[64:67], v[132:135], a[152:155], v[64:67]\n"
        "v_mfma_f32_16x16x32_bf16 v[68:71], v[128:131], a[140:143], v[68:71]\n"
        "v_mfma_f32_16x16x32_bf16 v[68:71], v[132:135], a[156:159], v[68:71]\n"
        "s_waitcnt vmcnt(12)\n"
        "v_mfma_f32_16x16x32_bf16 v[56:59], v[136:139], a[160:163], v[56:59]\n"
        "global_load_dwordx4 v[128:131], v196, s[78:79] offset:0 sc1\n"
        "global_load_dwordx4 v[132:135], v196, s[78:79] offset:1024 sc1\n"
        "v_mfma_f32_16x16x32_bf16 v[56:59], v[140:143], a[176:179], v[56:59]\n"
        "v_mfma_f32_16x16x32_bf16 v[60:63], v[136:139], a[164:167], v[60:63]\n"
        "v_mfma_f32_16x16x32_bf16 v[60:63], v[140:143], a[180:183], v[60:63]\n"
        "v_mfma_f32_16x16x32_bf16 v[64:67], v[136:139], a[168:171], v[64:67]\n"
        "v_mfma_f32_16x16x32_bf16 v[64:67], v[140:143], a[184:187], v[64:67]\n"
        "v_mfma_f32_16x16x32_bf16 v[68:71], v[136:139], a[172:175], v[68:71]\n"
        "v_mfma_f32_16x16x32_bf16 v[68:71], v[140:143], a[188:191], v[68:71]\n"
        "s_waitcnt vmcnt(12)\n"
        "v_mfma_f32_16x16x32_bf16 v[56:59], v[144:147], a[192:195], v[56:59]\n"
        "global_load_dwordx4 v[136:139], v197, s[78:79] offset:0 sc1\n"
        "global_load_dwordx4 v[140:143], v197, s[78:79] offset:1024 sc1\n"
        "v_mfma_f32_16x16x32_bf16 v[56:59], v[148:151], a[208:211], v[56:59]\n"
        "global_load_dwordx4 v[240:243], v44, s[38:39] offset:0 sc1\n"
        "global_load_dwordx4 v[244:247], v44, s[38:39] offset:1024 sc1\n"
        "v_mfma_f32_16x16x32_bf16 v[60:63], v[144:147], a[196:199], v[60:63]\n"
        "v_mfma_f32_16x16x32_bf16 v[60:63], v[148:151], a[212:215], v[60:63]\n"
        "v_mfma_f32_16x16x32_bf16 v[64:67], v[144:147], a[200:203], v[64:67]\n"
        "v_mfma_f32_16x16x32_bf16 v[64:67], v[148:151], a[216:219], v[64:67]\n"
        "v_mfma_f32_16x16x32_bf16 v[68:71], v[144:147], a[204:207], v[68:71]\n"
        "v_mfma_f32_16x16x32_bf16 v[68:71], v[148:151], a[220:223], v[68:71]\n"
        "s_waitcnt vmcnt(14)\n"
        "v_mfma_f32_16x16x32_bf16 v[56:59], v[152:155], a[224:227], v[56:59]\n"
        "global_load_dwordx4 v[144:147], v198, s[78:79] offset:0 sc1\n"
        "global_load_dwordx4 v[148:151], v198, s[78:79] offset:1024 sc1\n"
        "v_mfma_f32_16x16x32_bf16 v[56:59], v[156:159], a[240:243], v[56:59]\n"
        "v_mfma_f32_16x16x32_bf16 v[60:63], v[152:155], a[228:231], v[60:63]\n"
        "v_mfma_f32_16x16x32_bf16 v[60:63], v[156:159], a[244:247], v[60:63]\n"
        "v_mfma_f32_16x16x32_bf16 v[64:67], v[152:155], a[232:235], v[64:67]\n"
        "v_mfma_f32_16x16x32_bf16 v[64:67], v[156:159], a[248:251], v[64:67]\n"
        "v_mfma_f32_16x16x32_bf16 v[68:71], v[152:155], a[236:239], v[68:71]\n"
        "v_mfma_f32_16x16x32_bf16 v[68:71], v[156:159], a[252:255], v[68:71]\n"
        "s_waitcnt vmcnt(14)\n"
        "v_mfma_f32_16x16x32_bf16 v[72:75], v[96:99], a[0:3], 0\n"
        "global_load_dwordx4 v[152:155], v199, s[78:79] offset:0 sc1\n"
        "global_load_dwordx4 v[156:159], v199, s[78:79] offset:1024 sc1\n"
        "s_add_u32 s78, s78, 0x10080\n"
        "s_addc_u32 s79, s79, 0\n"
        "v_mfma_f32_16x16x32_bf16 v[72:75], v[100:103], a[16:19], v[72:75]\n"
        "v_mfma_f32_16x16x32_bf16 v[76:79], v[96:99], a[4:7], 0\n"
        "v_mfma_f32_16x16x32_bf16 v[76:79], v[100:103], a[20:23], v[76:79]\n"
        "v_mfma_f32_16x16x32_bf16 v[80:83], v[96:99], a[8:11], 0\n"
        "ds_write_b128 v47, v[60:63] offset:0\n"
        "ds_write_b128 v47, v[64:67] offset:1024\n"
        "v_mfma_f32_16x16x32_bf16 v[80:83], v[100:103], a[24:27], v[80:83]\n"
        "ds_write_b128 v47, v[68:71] offset:2048\n"
        "v_mfma_f32_16x16x32_bf16 v[84:87], v[96:99], a[12:15], 0\n"
        "v_mfma_f32_16x16x32_bf16 v[84:87], v[100:103], a[28:31], v[84:87]\n"
        "s_waitcnt vmcnt(14)\n"
        "v_mfma_f32_16x16x32_bf16 v[72:75], v[104:107], a[32:35], v[72:75]\n"
        "global_load_dwordx4 v[96:99], v192, s[78:79] offset:0 sc1\n"
        "global_load_dwordx4 v[100:103], v192, s[78:79] offset:1024 sc1\n"
        "v_mfma_f32_16x16x32_bf16 v[72:75], v[108:111], a[48:51], v[72:75]\n"
        "v_mfma_f32_16x16x32_bf16 v[76:79], v[104:107], a[36:39], v[76:79]\n"
        "v_mfma_f32_16x16x32_bf16 v[76:79], v[108:111], a[52:55], v[76:79]\n"
        "v_mfma_f32_16x16x32_bf16 v[80:83], v[104:107], a[40:43], v[80:83]\n"
        "s_waitcnt lgkmcnt(0)\n"
        "s_barrier\n"
        "v_mfma_f32_16x16x32_bf16 v[80:83], v[108:111], a[56:59], v[80:83]\n"
        "ds_read_b128 v[4:7], v48 offset:0\n"
        "ds_read_b128 v[8:11], v49 offset:0\n"
        "v_mfma_f32_16x16x32_bf16 v[84:87], v[104:107], a[44:47], v[84:87]\n"
        "ds_read_b128 v[12:15], v50 offset:0\n"
        "v_mfma_f32_16x16x32_bf16 v[84:87], v[108:111], a[60:63], v[84:87]\n"
        "s_waitcnt vmcnt(14)\n"
        "v_mfma_f32_16x16x32_bf16 v[72:75], v[112:115], a[64:67], v[72:75]\n"
        "global_load_dwordx4 v[104:107], v193, s[78:79] offset:0 sc1\n"
        "global_load_dwordx4 v[108:111], v193, s[78:79] offset:1024 sc1\n"
        "v_mfma_f32_16x16x32_bf16 v[72:75], v[116:119], a[80:83], v[72:75]\n"
        "v_mfma_f32_16x16x32_bf16 v[76:79], v[112:115], a[68:71], v[76:79]\n"
        "v_mfma_f32_16x16x32_bf16 v[76:79], v[116:119], a[84:87], v[76:79]\n"
        "v_mfma_f32_16x16x32_bf16 v[80:83], v[112:115], a[72:75], v[80:83]\n"
        "s_waitcnt lgkmcnt(0)\n"
        "v_add_f32 v4, v4, v8\n"
        "v_mfma_f32_16x16x32_bf16 v[80:83], v[116:119], a[88:91], v[80:83]\n"
        "v_add_f32 v5, v5, v9\n"
        "v_add_f32 v6, v6, v10\n"
        "v_mfma_f32_16x16x32_bf16 v[84:87], v[112:115], a[76:79], v[84:87]\n"
        "v_add_f32 v7, v7, v11\n"
        "v_add_f32 v212, v56, v12\n"
        "v_mfma_f32_16x16x32_bf16 v[84:87], v[116:119], a[92:95], v[84:87]\n"
        "v_add_f32 v213, v57, v13\n"
        "s_waitcnt vmcnt(14)\n"
        "v_mfma_f32_16x16x32_bf16 v[72:75], v[120:123], a[96:99], v[72:75]\n"
        "global_load_dwordx4 v[112:115], v194, s[78:79] offset:0 sc1\n"
        "global_load_dwordx4 v[116:119], v194, s[78:79] offset:1024 sc1\n"
        "v_mfma_f32_16x16x32_bf16 v[72:75], v[124:127], a[112:115], v[72:75]\n"
        "v_add_f32 v214, v58, v14\n"
        "v_add_f32 v215, v59, v15\n"
        "v_mfma_f32_16x16x32_bf16 v[76:79], v[120:123], a[100:103], v[76:79]\n"
        "v_add_f32 v212, v212, v4\n"
        "v_add_f32 v213, v213, v5\n"
        "v_mfma_f32_16x16x32_bf16 v[76:79], v[124:127], a[116:119], v[76:79]\n"
        "v_add_f32 v214, v214, v6\n"
        "v_add_f32 v215, v215, v7\n"
        "v_mfma_f32_16x16x32_bf16 v[80:83], v[120:123], a[104:107], v[80:83]\n"
        "v_mfma_f32_16x16x32_bf16 v[80:83], v[124:127], a[120:123], v[80:83]\n"
        "v_mfma_f32_16x16x32_bf16 v[84:87], v[120:123], a[108:111], v[84:87]\n"
        "v_mfma_f32_16x16x32_bf16 v[84:87], v[124:127], a[124:127], v[84:87]\n"
        "s_waitcnt vmcnt(14)\n"
        "v_mfma_f32_16x16x32_bf16 v[72:75], v[128:131], a[128:131], v[72:75]\n"
        "global_load_dwordx4 v[120:123], v195, s[78:79] offset:0 sc1\n"
        "global_load_dwordx4 v[124:127], v195, s[78:79] offset:1024 sc1\n"
        "v_mfma_f32_16x16x32_bf16 v[72:75], v[132:135], a[144:147], v[72:75]\n"
        "v_mfma_f32_16x16x32_bf16 v[76:79], v[128:131], a[132:135], v[76:79]\n"
        "v_mfma_f32_16x16x32_bf16 v[76:79], v[132:135], a[148:151], v[76:79]\n"
        "v_mfma_f32_16x16x32_bf16 v[80:83], v[128:131], a[136:139], v[80:83]\n"
        "v_mfma_f32_16x16x32_bf16 v[80:83], v[132:135], a[152:155], v[80:83]\n"
        "v_mfma_f32_16x16x32_bf16 v[84:87], v[128:131], a[140:143], v[84:87]\n"
        "v_mfma_f32_16x16x32_bf16 v[84:87], v[132:135], a[156:159], v[84:87]\n"
        "s_waitcnt vmcnt(14)\n"
        "v_mfma_f32_16x16x32_bf16 v[72:75], v[136:139], a[160:163], v[72:75]\n"
        "global_load_dwordx4 v[128:131], v196, s[78:79] offset:0 sc1\n"
        "global_load_dwordx4 v[132:135], v196, s[78:79] offset:1024 sc1\n"
        "v_mfma_f32_16x16x32_bf16 v[72:75], v[140:143], a[176:179], v[72:75]\n"
        "v_mfma_f32_16x16x32_bf16 v[76:79], v[136:139], a[164:167], v[76:79]\n"
        "v_mfma_f32_16x16x32_bf16 v[76:79], v[140:143], a[180:183], v[76:79]\n"
        "v_mfma_f32_16x16x32_bf16 v[80:83], v[136:139], a[168:171], v[80:83]\n"
        "v_mfma_f32_16x16x32_bf16 v[80:83], v[140:143], a[184:187], v[80:83]\n"
        "v_mfma_f32_16x16x32_bf16 v[84:87], v[136:139], a[172:175], v[84:87]\n"
        "v_mfma_f32_16x16x32_bf16 v[84:87], v[140:143], a[188:191], v[84:87]\n"
        "s_waitcnt vmcnt(12)\n"
        "v_mfma_f32_16x16x32_bf16 v[72:75], v[144:147], a[192:195], v[72:75]\n"
        "global_load_dwordx4 v[136:139], v197, s[78:79] offset:0 sc1\n"
        "global_load_dwordx4 v[140:143], v197, s[78:79] offset:1024 sc1\n"
        "v_mfma_f32_16x16x32_bf16 v[72:75], v[148:151], a[208:211], v[72:75]\n"
        "s_mov_b32 s53, 0x40000\n"
        "Lib_22:\n"
        "v_max3_u32 v54, v240, v241, v242\n"
        "v_max3_u32 v54, v54, v243, v244\n"
        "v_max3_u32 v54, v54, v245, v246\n"
        "v_max_u32 v54, v54, v247\n"
        "v_cmp_eq_u32 vcc, -1, v54\n"
        "s_nop 4\n"
        "s_cbranch_vccz Libok_23\n"
        "global_load_dwordx4 v[240:243], v44, s[38:39] offset:0 sc1\n"
        "global_load_dwordx4 v[244:247], v44, s[38:39] offset:1024 sc1\n"
        "s_waitcnt vmcnt(0)\n"
        "s_sub_u32 s53, s53, 1\n"
        "s_cbranch_scc0 Lib_22\n"
        "Libok_23:\n"
        "v_mfma_f32_16x16x32_bf16 v[76:79], v[144:147], a[196:199], v[76:79]\n"
        "v_mfma_f32_16x16x32_bf16 v[76:79], v[148:151], a[212:215], v[76:79]\n"
        "v_mfma_f32_16x16x32_bf16 v[80:83], v[144:147], a[200:203], v[80:83]\n"
        "v_lshlrev_b32 v54, 16, v240\n"
        "v_and_b32 v240, 0xffff0000, v240\n"
        "v_mfma_f32_16x16x32_bf16 v[80:83], v[148:151], a[216:219], v[80:83]\n"
        "v_lshlrev_b32 v55, 16, v241\n"
        "v_and_b32 v241, 0xffff0000, v241\n"
        "v_mfma_f32_16x16x32_bf16 v[84:87], v[144:147], a[204:207], v[84:87]\n"
        "v_mfma_f32_16x16x32_bf16 v[84:87], v[148:151], a[220:223], v[84:87]\n"
        "s_waitcnt vmcnt(12)\n"
        "v_mfma_f32_16x16x32_bf16 v[72:75], v[152:155], a[224:227], v[72:75]\n"
        "global_load_dwordx4 v[144:147], v198, s[78:79] offset:0 sc1\n"
        "global_load_dwordx4 v[148:151], v198, s[78:79] offset:1024 sc1\n"
        "v_mfma_f32_16x16x32_bf16 v[72:75], v[156:159], a[240:243], v[72:75]\n"
        "v_sub_f32 v208, v54, v208\n"
        "v_sub_f32 v209, v240, v209\n"
        "v_mfma_f32_16x16x32_bf16 v[76:79], v[152:155], a[228:231], v[76:79]\n"
        "v_sub_f32 v210, v55, v210\n"
        "v_sub_f32 v211, v241, v211\n"
        "v_mfma_f32_16x16x32_bf16 v[76:79], v[156:159], a[244:247], v[76:79]\n"
        "v_mfma_f32_16x16x32_bf16 v[80:83], v[152:155], a[232:235], v[80:83]\n"
        "v_cvt_pk_bf16_f32 v208, v208, v209\n"
        "v_cvt_pk_bf16_f32 v209, v210, v211\n"
        "v_mfma_f32_16x16x32_bf16 v[80:83], v[156:159], a[248:251], v[80:83]\n"
        "v_lshlrev_b32 v54, 16, v242\n"
        "v_and_b32 v242, 0xffff0000, v242\n"
        "v_mfma_f32_16x16x32_bf16 v[84:87], v[152:155], a[236:239], v[84:87]\n"
        "v_mfma_f32_16x16x32_bf16 v[84:87], v[156:159], a[252:255], v[84:87]\n"
        "s_waitcnt vmcnt(12)\n"
        "v_mfma_f32_16x16x32_bf16 v[56:59], v[96:99], a[0:3], 0\n"
        "global_load_dwordx4 v[152:155], v199, s[78:79] offset:0 sc1\n"
        "global_load_dwordx4 v[156:159], v199, s[78:79] offset:1024 sc1\n"
        "v_mfma_f32_16x16x32_bf16 v[56:59], v[100:103], a[16:19], v[56:59]\n"
        "v_lshlrev_b32 v55, 16, v243\n"
        "v_and_b32 v243, 0xffff0000, v243\n"
        "v_mfma_f32_16x16x32_bf16 v[60:63], v[96:99], a[4:7], 0\n"
        "v_sub_f32 v212, v54, v212\n"
        "v_sub_f32 v213, v242, v213\n"
        "v_mfma_f32_16x16x32_bf16 v[60:63], v[100:103], a[20:23], v[60:63]\n"
        "v_mfma_f32_16x16x32_bf16 v[64:67], v[96:99], a[8:11], 0\n"
        "ds_write_b128 v47, v[76:79] offset:3072\n"
        "ds_write_b128 v47, v[80:83] offset:4096\n"
        "v_mfma_f32_16x16x32_bf16 v[64:67], v[100:103], a[24:27], v[64:67]\n"
        "ds_write_b128 v47, v[84:87] offset:5120\n"
        "v_sub_f32 v214, v55, v214\n"
        "v_mfma_f32_16x16x32_bf16 v[68:71], v[96:99], a[12:15], 0\n"
        "v_sub_f32 v215, v243, v215\n"
        "v_cvt_pk_bf16_f32 v212, v212, v213\n"
        "v_mfma_f32_16x16x32_bf16 v[68:71], v[100:103], a[28:31], v[68:71]\n"
        "v_cvt_pk_bf16_f32 v213, v214, v215\n"
        "s_waitcnt vmcnt(12)\n"
        "v_mfma_f32_16x16x32_bf16 v[56:59], v[104:107], a[32:35], v[56:59]\n"
        "v_mov_b32 v210, v212\n"
        "v_mov_b32 v211, v213\n"
        "s_nop 1\n"
        "v_mfma_f32_16x16x32_bf16 v[56:59], v[108:111], a[48:51], v[56:59]\n"
        "v_permlane32_swap_b32 v208, v210\n"
        "v_permlane32_swap_b32 v209, v211\n"
        "s_nop 1\n"
        "v_permlane16_swap_b32 v208, v210\n"
        "v_permlane16_swap_b32 v209, v211\n"
        "s_nop 1\n"
        "v_mfma_f32_16x16x32_bf16 v[60:63], v[104:107], a[36:39], v[60:63]\n"
        "v_mfma_f32_16x16x32_bf16 v[60:63], v[108:111], a[52:55], v[60:63]\n"
        "v_mfma_f32_16x16x32_bf16 v[64:67], v[104:107], a[40:43], v[64:67]\n"
        "s_waitcnt lgkmcnt(0)\n"
        "s_barrier\n"
        "v_mfma_f32_16x16x32_bf16 v[64:67], v[108:111], a[56:59], v[64:67]\n"
        "ds_read_b128 v[4:7], v48 offset:3072\n"
        "ds_read_b128 v[8:11], v49 offset:3072\n"
        "v_mfma_f32_16x16x32_bf16 v[68:71], v[104:107], a[44:47], v[68:71]\n"
        "ds_read_b128 v[12:15], v50 offset:3072\n"
        "global_store_dwordx4 v52, v[208:211], s[34:35] offset:160 sc1\n"
        "v_mfma_f32_16x16x32_bf16 v[68:71], v[108:111], a[60:63], v[68:71]\n"
        "s_waitcnt vmcnt(11)\n"
        "v_mfma_f32_16x16x32_bf16 v[56:59], v[112:115], a[64:67], v[56:59]\n"
        "v_mfma_f32_16x16x32_bf16 v[56:59], v[116:119], a[80:83], v[56:59]\n"
        "v_mfma_f32_16x16x32_bf16 v[60:63], v[112:115], a[68:71], v[60:63]\n"
        "v_mfma_f32_16x16x32_bf16 v[60:63], v[116:119], a[84:87], v[60:63]\n"
        "v_mfma_f32_16x16x32_bf16 v[64:67], v[112:115], a[72:75], v[64:67]\n"
        "s_waitcnt lgkmcnt(0)\n"
        "v_add_f32 v4, v4, v8\n"
        "v_mfma_f32_16x16x32_bf16 v[64:67], v[116:119], a[88:91], v[64:67]\n"
        "v_add_f32 v5, v5, v9\n"
        "v_add_f32 v6, v6, v10\n"
        "v_mfma_f32_16x16x32_bf16 v[68:71], v[112:115], a[76:79], v[68:71]\n"
        "v_add_f32 v7, v7, v11\n"
        "v_add_f32 v216, v72, v12\n"
        "v_mfma_f32_16x16x32_bf16 v[68:71], v[116:119], a[92:95], v[68:71]\n"
        "v_add_f32 v217, v73, v13\n"
        "s_waitcnt vmcnt(9)\n"
        "v_mfma_f32_16x16x32_bf16 v[56:59], v[120:123], a[96:99], v[56:59]\n"
        "v_add_f32 v218, v74, v14\n"
        "v_add_f32 v219, v75, v15\n"
        "v_mfma_f32_16x16x32_bf16 v[56:59], v[124:127], a[112:115], v[56:59]\n"
        "v_add_f32 v216, v216, v4\n"
        "v_add_f32 v217, v217, v5\n"
        "v_mfma_f32_16x16x32_bf16 v[60:63], v[120:123], a[100:103], v[60:63]\n"
        "v_add_f32 v218, v218, v6\n"
        "v_add_f32 v219, v219, v7\n"
        "v_mfma_f32_16x16x32_bf16 v[60:63], v[124:127], a[116:119], v[60:63]\n"
        "v_mfma_f32_16x16x32_bf16 v[64:67], v[120:123], a[104:107], v[64:67]\n"
        "v_lshlrev_b32 v54, 16, v244\n"
        "v_and_b32 v244, 0xffff0000, v244\n"
        "v_mfma_f32_16x16x32_bf16 v[64:67], v[124:127], a[120:123], v[64:67]\n"
        "v_lshlrev_b32 v55, 16, v245\n"
        "v_and_b32 v245, 0xffff0000, v245\n"
        "v_mfma_f32_16x16x32_bf16 v[68:71], v[120:123], a[108:111], v[68:71]\n"
        "v_mfma_f32_16x16x32_bf16 v[68:71], v[124:127], a[124:127], v[68:71]\n"
        "s_waitcnt vmcnt(7)\n"
        "v_mfma_f32_16x16x32_bf16 v[60:63], v[128:131], a[132:135], v[60:63]\n"
        "v_sub_f32 v216, v54, v216\n"
        "v_sub_f32 v217, v244, v217\n"
        "v_mfma_f32_16x16x32_bf16 v[60:63], v[132:135], a[148:151], v[60:63]\n"
        "v_sub_f32 v218, v55, v218\n"
        "v_sub_f32 v219, v245, v219\n"
        "v_mfma_f32_16x16x32_bf16 v[64:67], v[128:131], a[136:139], v[64:67]\n"
        "v_cvt_pk_bf16_f32 v216, v216, v217\n"
        "v_cvt_pk_bf16_f32 v217, v218, v219\n"
        "v_mfma_f32_16x16x32_bf16 v[64:67], v[132:135], a[152:155], v[64:67]\n"
        "v_mfma_f32_16x16x32_bf16 v[68:71], v[128:131], a[140:143], v[68:71]\n"
        "v_mfma_f32_16x16x32_bf16 v[68:71], v[132:135], a[156:159], v[68:71]\n"
        "s_waitcnt vmcnt(5)\n"
        "v_mfma_f32_16x16x32_bf16 v[60:63], v[136:139], a[164:167], v[60:63]\n"
        "v_mfma_f32_16x16x32_bf16 v[60:63], v[140:143], a[180:183], v[60:63]\n"
        "v_mfma_f32_16x16x32_bf16 v[64:67], v[136:139], a[168:171], v[64:67]\n"
        "v_mfma_f32_16x16x32_bf16 v[64:67], v[140:143], a[184:187], v[64:67]\n"
        "v_mfma_f32_16x16x32_bf16 v[68:71], v[136:139], a[172:175], v[68:71]\n"
        "v_mfma_f32_16x16x32_bf16 v[68:71], v[140:143], a[188:191], v[68:71]\n"
        "s_waitcnt vmcnt(3)\n"
        "v_mfma_f32_16x16x32_bf16 v[60:63], v[144:147], a[196:199], v[60:63]\n"
        "v_mfma_f32_16x16x32_bf16 v[60:63], v[148:151], a[212:215], v[60:63]\n"
        "v_mfma_f32_16x16x32_bf16 v[64:67], v[144:147], a[200:203], v[64:67]\n"
        "v_mfma_f32_16x16x32_bf16 v[64:67], v[148:151], a[216:219], v[64:67]\n"
        "v_mfma_f32_16x16x32_bf16 v[68:71], v[144:147], a[204:207], v[68:71]\n"
        "v_mfma_f32_16x16x32_bf16 v[68:71], v[148:151], a[220:223], v[68:71]\n"
        "s_waitcnt vmcnt(1)\n"
        "v_mfma_f32_16x16x32_bf16 v[60:63], v[152:155], a[228:231], v[60:63]\n"
        "v_mfma_f32_16x16x32_bf16 v[60:63], v[156:159], a[244:247], v[60:63]\n"
        "v_mfma_f32_16x16x32_bf16 v[64:67], v[152:155], a[232:235], v[64:67]\n"
        "v_mfma_f32_16x16x32_bf16 v[64:67], v[156:159], a[248:251], v[64:67]\n"
        "v_mfma_f32_16x16x32_bf16 v[68:71], v[152:155], a[236:239], v[68:71]\n"
        "v_mfma_f32_16x16x32_bf16 v[68:71], v[156:159], a[252:255], v[68:71]\n"
        "v_mfma_f32_16x16x32_bf16 v[56:59], v[128:131], a[128:131], v[56:59]\n"
        "v_lshlrev_b32 v220, 16, v246\n"
        "v_and_b32 v221, 0xffff0000, v246\n"
        "v_mfma_f32_16x16x32_bf16 v[56:59], v[132:135], a[144:147], v[56:59]\n"
        "v_lshlrev_b32 v222, 16, v247\n"
        "v_and_b32 v223, 0xffff0000, v247\n"
        "v_mfma_f32_16x16x32_bf16 v[56:59], v[136:139], a[160:163], v[56:59]\n"
        "s_nop 0\n"
        "ds_write_b128 v47, v[60:63] offset:0\n"
        "ds_write_b128 v47, v[64:67] offset:1024\n"
        "v_mfma_f32_16x16x32_bf16 v[56:59], v[140:143], a[176:179], v[56:59]\n"
        "ds_write_b128 v47, v[68:71] offset:2048\n"
        "v_mfma_f32_16x16x32_bf16 v[56:59], v[144:147], a[192:195], v[56:59]\n"
        "v_mfma_f32_16x16x32_bf16 v[56:59], v[148:151], a[208:211], v[56:59]\n"
        "v_mfma_f32_16x16x32_bf16 v[56:59], v[152:155], a[224:227], v[56:59]\n"
        "s_waitcnt lgkmcnt(0)\n"
        "s_barrier\n"
        "ds_read_b128 v[4:7], v48 offset:0\n"
        "ds_read_b128 v[8:11], v49 offset:0\n"
        "ds_read_b128 v[12:15], v50 offset:0\n"
        "v_mfma_f32_16x16x32_bf16 v[56:59], v[156:159], a[240:243], v[56:59]\n"
        "s_waitcnt lgkmcnt(0)\n"
        "v_add_f32 v4, v4, v8\n"
        "v_add_f32 v5, v5, v9\n"
        "v_add_f32 v6, v6, v10\n"
        "v_add_f32 v7, v7, v11\n"
        "v_sub_f32 v220, v12, v220\n"
        "v_sub_f32 v221, v13, v221\n"
        "v_sub_f32 v222, v14, v222\n"
        "v_sub_f32 v223, v15, v223\n"
        "v_add_f32 v220, v220, v4\n"
        "v_add_f32 v221, v221, v5\n"
        "v_add_f32 v222, v222, v6\n"
        "v_add_f32 v223, v223, v7\n"
        "v_add_f32 v220, v220, v56\n"
        "v_add_f32 v221, v221, v57\n"
        "v_add_f32 v222, v222, v58\n"
        "v_add_f32 v223, v223, v59\n"
        "v_cvt_pk_bf16_f32 v220, -v220, -v221\n"
        "v_cvt_pk_bf16_f32 v221, -v222, -v223\n"
        "v_mov_b32 v218, v220\n"
        "v_mov_b32 v219, v221\n"
        "s_nop 1\n"
        "v_permlane32_swap_b32 v216, v218\n"
        "v_permlane32_swap_b32 v217, v219\n"
        "s_nop 1\n"
        "v_permlane16_swap_b32 v216, v218\n"
        "v_permlane16_swap_b32 v217, v219\n"
        "s_nop 1\n"
        "global_store_dwordx4 v52, v[216:219], s[34:35] offset:224 sc1\n"
        "s_endpgm\n"
        "Lend:\n"
        :
        : "s"(__builtin_amdgcn_kernarg_segment_ptr()), "s"(blockIdx.x), "v"(threadIdx.x)
        : "memory", "vcc", "scc", "s8", "s9", "s10", "s11", "s12", "s13", "s14", "s15", "s16", "s17", "s18", "s19", "s20", "s21", "s22", "s23", "s24", "s25", "s26", "s27", "s28", "s29", "s30", "s31", "s34", "s35", "s36", "s37", "s38", "s39", "s40", "s41", "s42", "s43", "s44", "s45", "s46", "s47", "s48", "s49", "s50", "s51", "s52", "s53", "s54", "s55", "s56", "s57", "s58", "s59", "s60", "s61", "s62", "s63", "s64", "s65", "s66", "s67", "s68", "s69", "s70", "s71", "s72", "s73", "s74", "s75", "s76", "s77", "s78", "s79", "s80", "s81", "s82", "s83", "s84", "s85", "s86", "s87", "s88", "s89", "s90", "s91", "s92", "s93", "s94", "s95", "s96", "s97", "s98", "s99",
          "v1", "v2", "v3", "v4", "v5", "v6", "v7", "v8", "v9", "v10", "v11", "v12", "v13", "v14", "v15", "v16", "v17", "v18", "v19", "v20", "v21", "v22", "v23", "v24", "v25", "v26", "v27", "v28", "v29", "v30", "v31", "v32", "v33", "v34", "v35", "v36", "v37", "v38", "v39", "v40", "v41", "v42", "v43", "v44", "v45", "v46", "v47", "v48", "v49", "v50", "v51", "v52", "v53", "v54", "v55", "v56", "v57", "v58", "v59", "v60", "v61", "v62", "v63", "v64", "v65", "v66", "v67", "v68", "v69", "v70", "v71", "v72", "v73", "v74", "v75", "v76", "v77", "v78", "v79", "v80", "v81", "v82", "v83", "v84", "v85", "v86", "v87", "v88", "v89", "v90", "v91", "v92", "v93", "v94", "v95", "v96", "v97", "v98", "v99", "v100", "v101", "v102", "v103", "v104", "v105", "v106", "v107", "v108", "v109", "v110", "v111", "v112", "v113", "v114", "v115", "v116", "v117", "v118", "v119", "v120", "v121", "v122", "v123", "v124", "v125", "v126", "v127", "v128", "v129", "v130", "v131", "v132", "v133", "v134", "v135", "v136", "v137", "v138", "v139", "v140", "v141", "v142", "v143", "v144", "v145", "v146", "v147", "v148", "v149", "v150", "v151", "v152", "v153", "v154", "v155", "v156", "v157", "v158", "v159", "v160", "v161", "v162", "v163", "v164", "v165", "v166", "v167", "v168", "v169", "v170", "v171", "v172", "v173", "v174", "v175", "v176", "v177", "v178", "v179", "v180", "v181", "v182", "v183", "v184", "v185", "v186", "v187", "v188", "v189", "v190", "v191", "v192", "v193", "v194", "v195", "v196", "v197", "v198", "v199", "v200", "v201", "v202", "v203", "v204", "v205", "v206", "v207", "v208", "v209", "v210", "v211", "v212", "v213", "v214", "v215", "v216", "v217", "v218", "v219", "v220", "v221", "v222", "v223", "v224", "v225", "v226", "v227", "v228", "v229", "v230", "v231", "v232", "v233", "v234", "v235", "v236", "v237", "v238", "v239", "v240", "v241", "v242", "v243", "v244", "v245", "v246", "v247", "v248", "v249", "v250", "v251", "v252", "v253", "v254", "v255",
          "a0", "a1", "a2", "a3", "a4", "a5", "a6", "a7", "a8", "a9", "a10", "a11", "a12", "a13", "a14", "a15", "a16", "a17", "a18", "a19", "a20", "a21", "a22", "a23", "a24", "a25", "a26", "a27", "a28", "a29", "a30", "a31", "a32", "a33", "a34", "a35", "a36", "a37", "a38", "a39", "a40", "a41", "a42", "a43", "a44", "a45", "a46", "a47", "a48", "a49", "a50", "a51", "a52", "a53", "a54", "a55", "a56", "a57", "a58", "a59", "a60", "a61", "a62", "a63", "a64", "a65", "a66", "a67", "a68", "a69", "a70", "a71", "a72", "a73", "a74", "a75", "a76", "a77", "a78", "a79", "a80", "a81", "a82", "a83", "a84", "a85", "a86", "a87", "a88", "a89", "a90", "a91", "a92", "a93", "a94", "a95", "a96", "a97", "a98", "a99", "a100", "a101", "a102", "a103", "a104", "a105", "a106", "a107", "a108", "a109", "a110", "a111", "a112", "a113", "a114", "a115", "a116", "a117", "a118", "a119", "a120", "a121", "a122", "a123", "a124", "a125", "a126", "a127", "a128", "a129", "a130", "a131", "a132", "a133", "a134", "a135", "a136", "a137", "a138", "a139", "a140", "a141", "a142", "a143", "a144", "a145", "a146", "a147", "a148", "a149", "a150", "a151", "a152", "a153", "a154", "a155", "a156", "a157", "a158", "a159", "a160", "a161", "a162", "a163", "a164", "a165", "a166", "a167", "a168", "a169", "a170", "a171", "a172", "a173", "a174", "a175", "a176", "a177", "a178", "a179", "a180", "a181", "a182", "a183", "a184", "a185", "a186", "a187", "a188", "a189", "a190", "a191", "a192", "a193", "a194", "a195", "a196", "a197", "a198", "a199", "a200", "a201", "a202", "a203", "a204", "a205", "a206", "a207", "a208", "a209", "a210", "a211", "a212", "a213", "a214", "a215", "a216", "a217", "a218", "a219", "a220", "a221", "a222", "a223", "a224", "a225", "a226", "a227", "a228", "a229", "a230", "a231", "a232", "a233", "a234", "a235", "a236", "a237", "a238", "a239", "a240", "a241", "a242", "a243", "a244", "a245", "a246", "a247", "a248", "a249", "a250", "a251", "a252", "a253", "a254", "a255");
}

extern "C" void kernel_launch(void* const* d_in, const int* in_sizes, int n_in,
                              void* d_out, int out_size, void* d_ws, size_t ws_size,
                              hipStream_t stream) {
    (void)hipFuncSetAttribute((const void*)fnet_fused, hipFuncAttributeMaxDynamicSharedMemorySize, 163840);
    fnet_fused<<<256, 256, 163840, stream>>>(d_in[0], d_in[1], d_in[2], d_in[3], d_in[4], d_out, d_ws);
}
```
